```python
import torch

def _softmax(x, axis=-1):
    return torch.softmax(x, dim=axis)
def _gelu(x):
    return torch.nn.functional.gelu(x, approximate="tanh")
def _silu(x):
    return torch.nn.functional.silu(x)
def _sigmoid(x):
    return torch.sigmoid(x)
def _layernorm(x, axis=-1, eps=1e-5):
    m = x.mean(dim=axis, keepdim=True)
    v = x.var(dim=axis, keepdim=True, unbiased=False)
    return (x - m) / torch.sqrt(v + eps)
def _scatter_add(tgt, idx, src, axis=0):
    return tgt.clone().index_add_(axis, idx, src)
def _segment_sum(src, seg, n):
    out = torch.zeros((n,) + src.shape[1:], dtype=src.dtype)
    return out.index_add_(0, seg, src)
def _pad(x, pw):
    flat = tuple(v for pair in reversed(pw) for v in pair)
    return torch.nn.functional.pad(x, flat)

kernel_name = 'recurrent_scan__ssm_conv_s5__dense_activation_gelu__prompt'

def setup_inputs():

    _w = (lambda a: (lambda t: t.to(torch.bfloat16) if t.is_floating_point() else t)(torch.as_tensor(a)))
    def _A_setup():
        B, S, D = 1, 2048, 576
        P = D
        Lambda_re = -0.5 * (lambda _x: torch.exp(torch.as_tensor(_x)))((lambda *s: torch.randn(*s, dtype=torch.bfloat16) * 0.4)(P))
        Lambda_im = (lambda *s: torch.randn(*s, dtype=torch.bfloat16) * 0.8)(P) * 1.0
        B_re = (lambda *s: torch.randn(*s, dtype=torch.bfloat16) * 0.8)(P, D) * (D ** -0.5)
        B_im = (lambda *s: torch.randn(*s, dtype=torch.bfloat16) * 0.8)(P, D) * (D ** -0.5)
        C_re = (lambda *s: torch.randn(*s, dtype=torch.bfloat16) * 0.8)(D, P) * (P ** -0.5)
        C_im = (lambda *s: torch.randn(*s, dtype=torch.bfloat16) * 0.8)(D, P) * (P ** -0.5)
        log_dt = (lambda *s: torch.randn(*s, dtype=torch.bfloat16) * 0.4)(P) * 0.3 - 2.0
        Dskip = (lambda *s: torch.randn(*s, dtype=torch.bfloat16) * 0.2)(D)
        return {
            "x": _w((lambda *s: torch.randn(*s, dtype=torch.bfloat16) * 0.05)(B, S, D)),
            "Lambda_re": _w(Lambda_re),
            "Lambda_im": _w(Lambda_im),
            "B_re": _w(B_re),
            "B_im": _w(B_im),
            "C_re": _w(C_re),
            "C_im": _w(C_im),
            "log_dt": _w(log_dt),
            "Dskip": _w(Dskip),
        }
    _A_in = _A_setup()

    def _B_setup():
        B, S, D, F = 1, 2048, 576, 4608
        return {
            "x": _w((lambda *s: torch.randn(*s, dtype=torch.bfloat16) * 0.05)(B, S, D)),
            "w1": _w((lambda *s: torch.randn(*s, dtype=torch.bfloat16) * 0.8)(D, F) * (D ** -0.5)),
            "b1": _w((lambda *s: torch.randn(*s, dtype=torch.bfloat16) * 12.8)(F) * 0.01),
            "w2": _w((lambda *s: torch.randn(*s, dtype=torch.bfloat16) * 0.8)(F, D) * (F ** -0.5)),
            "b2": _w((lambda *s: torch.randn(*s, dtype=torch.bfloat16) * 6.4)(D) * 0.01),
        }
    _B_in = _B_setup()

    return {
        "A_x": _A_in["x"],
        "A_Lambda_re": _A_in["Lambda_re"],
        "A_Lambda_im": _A_in["Lambda_im"],
        "A_B_re": _A_in["B_re"],
        "A_B_im": _A_in["B_im"],
        "A_C_re": _A_in["C_re"],
        "A_C_im": _A_in["C_im"],
        "A_log_dt": _A_in["log_dt"],
        "A_Dskip": _A_in["Dskip"],
        "B_x": _B_in["x"],
        "B_w1": _B_in["w1"],
        "B_b1": _B_in["b1"],
        "B_w2": _B_in["w2"],
        "B_b2": _B_in["b2"],
    }

def reference(A_x, A_Lambda_re, A_Lambda_im, A_B_re, A_B_im, A_C_re, A_C_im, A_log_dt, A_Dskip, B_x, B_w1, B_b1, B_w2, B_b2):
    def _A_ref(x, Lambda_re, Lambda_im, B_re, B_im, C_re, C_im, log_dt, Dskip):
        Bsz, L, H = x.shape
        Lam = Lambda_re + 1j * Lambda_im
        Bmat = B_re + 1j * B_im
        Cmat = C_re + 1j * C_im
        dt = torch.exp(log_dt)
        Lam_bar = torch.exp(dt * Lam)
        B_bar = ((Lam_bar - 1.0) / Lam)[:, None] * Bmat
        Bu = torch.einsum("bld,pd->blp", x + 0j, B_bar)
        h = torch.zeros_like(Bu[:, 0, :])
        hs = []
        for t in range(L):
            h = Lam_bar[None, :] * h + Bu[:, t, :]
            hs.append(h)
        hseq = torch.stack(hs, 1)
        y = 2.0 * torch.einsum("blp,dp->bld", hseq, Cmat).real + x * Dskip[None, None, :]
        return y

    def _B_ref(x, w1, b1, w2, b2):
        h = torch.einsum("bsd,df->bsf", x, w1) + b1
        h = _gelu(h)
        return torch.einsum("bsf,fd->bsd", h, w2) + b2

    x = A_x
    xn = _layernorm(x, axis=-1)
    s = _A_ref(xn, A_Lambda_re, A_Lambda_im, A_B_re, A_B_im, A_C_re, A_C_im, A_log_dt, A_Dskip)
    g = _silu(_B_ref(xn, B_w1, B_b1, B_w2, B_b2))
    return x + s * g
```

```cpp
#include <hip/hip_runtime.h>
#include <hip/hip_bf16.h>
#include <stdint.h>

typedef unsigned short u16;
__device__ __forceinline__ float bf2f(u16 b){ return __uint_as_float(((unsigned)b)<<16); }
__device__ __forceinline__ u16 f2bf(float f){ unsigned u=__float_as_uint(f); u += 0x7fffu + ((u>>16)&1u); return (u16)(u>>16); }
__device__ __forceinline__ float rbf(float f){ return bf2f(f2bf(f)); }

constexpr int L=2048, D=576, P=576, F=4608;

__global__ void k_ln(const u16* x, u16* xn){
  int row=blockIdx.x; int lane=threadIdx.x;
  float v[9]; float s=0;
  for(int i=0;i<9;i++){ v[i]=bf2f(x[row*D+lane+64*i]); s+=v[i]; }
  for(int o=32;o;o>>=1) s+=__shfl_xor(s,o);
  float m=s/D; float q=0;
  for(int i=0;i<9;i++){ float d=v[i]-m; q+=d*d; }
  for(int o=32;o;o>>=1) q+=__shfl_xor(q,o);
  float var=rbf(q/D); float mb=rbf(m);
  float den=rbf(sqrtf(rbf(var+1e-5f)));
  for(int i=0;i<9;i++){ xn[row*D+lane+64*i]=f2bf(rbf(v[i]-mb)/den); }
}
__global__ void k_bu(const u16* xn,const u16* Bre,const u16* Bim,float* bu){
  int p=blockIdx.x*64+threadIdx.x; int t=blockIdx.y;
  float a=0,b=0;
  for(int d=0;d<D;d++){ float xv=bf2f(xn[t*D+d]); a+=xv*bf2f(Bre[p*D+d]); b+=xv*bf2f(Bim[p*D+d]); }
  bu[(t*P+p)*2]=a; bu[(t*P+p)*2+1]=b;
}
__global__ void k_scan(const u16* Lre,const u16* Lim,const u16* ldt,float* bu){
  int p=blockIdx.x*64+threadIdx.x;
  float lr=bf2f(Lre[p]), li=bf2f(Lim[p]); float dt=rbf(expf(bf2f(ldt[p])));
  float e=expf(dt*lr); float ar=e*cosf(dt*li), ai=e*sinf(dt*li);
  float nr=ar-1.f, ni=ai; float den=lr*lr+li*li;
  float cr=(nr*lr+ni*li)/den, ci=(ni*lr-nr*li)/den;
  float hr=0,hi=0;
  for(int t=0;t<L;t++){
    float a=bu[(t*P+p)*2], b=bu[(t*P+p)*2+1];
    float ur=cr*a-ci*b, ui=cr*b+ci*a;
    float nhr=ar*hr-ai*hi+ur, nhi=ar*hi+ai*hr+ui; hr=nhr; hi=nhi;
    bu[(t*P+p)*2]=hr; bu[(t*P+p)*2+1]=hi;
  }
}
__global__ void k_ffn1(const u16* xn,const u16* w1,const u16* b1,u16* h){
  int f=blockIdx.x*64+threadIdx.x; int t=blockIdx.y;
  float a=0;
  for(int d=0;d<D;d++) a+=bf2f(xn[t*D+d])*bf2f(w1[d*F+f]);
  a=rbf(rbf(a)+bf2f(b1[f]));
  float u=0.7978845608028654f*(a+0.044715f*a*a*a);
  h[t*F+f]=f2bf(0.5f*a*(1.f+tanhf(u)));
}
__global__ void k_out(const u16* x,const u16* xn,const u16* h,const u16* w2,const u16* b2,const float* hs,const u16* Cre,const u16* Cim,const u16* Dsk,float* out){
  int d=blockIdx.x*64+threadIdx.x; int t=blockIdx.y;
  float a=0;
  for(int f=0;f<F;f++) a+=bf2f(h[t*F+f])*bf2f(w2[f*D+d]);
  a=rbf(rbf(a)+bf2f(b2[d]));
  float g=rbf(a/(1.f+expf(-a)));
  float y=0;
  for(int p=0;p<P;p++) y+=hs[(t*P+p)*2]*bf2f(Cre[d*P+p])-hs[(t*P+p)*2+1]*bf2f(Cim[d*P+p]);
  float s=2.f*y+rbf(bf2f(xn[t*D+d])*bf2f(Dsk[d]));
  out[t*D+d]=bf2f(x[t*D+d])+s*g;
}

extern "C" void kernel_launch(void* const* d_in, const int* in_sizes, int n_in,
                              void* d_out, int out_size, void* d_ws, size_t ws_size,
                              hipStream_t stream) {
  const u16* x=(const u16*)d_in[0]; const u16* Lre=(const u16*)d_in[1]; const u16* Lim=(const u16*)d_in[2];
  const u16* Bre=(const u16*)d_in[3]; const u16* Bim=(const u16*)d_in[4]; const u16* Cre=(const u16*)d_in[5]; const u16* Cim=(const u16*)d_in[6];
  const u16* ldt=(const u16*)d_in[7]; const u16* Dsk=(const u16*)d_in[8];
  const u16* w1=(const u16*)d_in[10]; const u16* b1=(const u16*)d_in[11]; const u16* w2=(const u16*)d_in[12]; const u16* b2=(const u16*)d_in[13];
  char* ws=(char*)d_ws;
  u16* xn=(u16*)ws; float* bu=(float*)(ws+(4<<20)); u16* h=(u16*)(ws+(32<<20));
  k_ln<<<L,64,0,stream>>>(x,xn);
  k_bu<<<dim3(P/64,L),64,0,stream>>>(xn,Bre,Bim,bu);
  k_scan<<<P/64,64,0,stream>>>(Lre,Lim,ldt,bu);
  k_ffn1<<<dim3(F/64,L),64,0,stream>>>(xn,w1,b1,h);
  k_out<<<dim3(D/64,L),64,0,stream>>>(x,xn,h,w2,b2,bu,Cre,Cim,Dsk,(float*)d_out);
}
```

```cpp
#include <hip/hip_runtime.h>
#include <hip/hip_bf16.h>
#include <stdint.h>

typedef unsigned short u16;
__device__ __forceinline__ float bf2f(u16 b){ return __uint_as_float(((unsigned)b)<<16); }
__device__ __forceinline__ u16 f2bf(float f){ unsigned u=__float_as_uint(f); u += 0x7fffu + ((u>>16)&1u); return (u16)(u>>16); }
__device__ __forceinline__ float rbf(float f){ return bf2f(f2bf(f)); }

constexpr int L=2048, D=576, P=576, F=4608;

__global__ void k_ln(const u16* x, u16* xn){
  int row=blockIdx.x; int lane=threadIdx.x;
  float v[9]; float s=0;
  for(int i=0;i<9;i++){ v[i]=bf2f(x[row*D+lane+64*i]); s+=v[i]; }
  for(int o=32;o;o>>=1) s+=__shfl_xor(s,o);
  float m=s/D; float q=0;
  for(int i=0;i<9;i++){ float d=v[i]-m; q+=d*d; }
  for(int o=32;o;o>>=1) q+=__shfl_xor(q,o);
  float var=q/D; float mb=m;
  float den=sqrtf(var+1e-5f);
  for(int i=0;i<9;i++){ xn[row*D+lane+64*i]=f2bf((v[i]-mb)/den); }
}
__global__ void k_bu(const u16* xn,const u16* Bre,const u16* Bim,float* bu){
  int p=blockIdx.x*64+threadIdx.x; int t=blockIdx.y;
  float a=0,b=0;
  for(int d=0;d<D;d++){ float xv=bf2f(xn[t*D+d]); a+=xv*bf2f(Bre[p*D+d]); b+=xv*bf2f(Bim[p*D+d]); }
  bu[(t*P+p)*2]=a; bu[(t*P+p)*2+1]=b;
}
__global__ void k_scan(const u16* Lre,const u16* Lim,const u16* ldt,float* bu){
  int p=blockIdx.x*64+threadIdx.x;
  float lr=bf2f(Lre[p]), li=bf2f(Lim[p]); float dt=expf(bf2f(ldt[p]));
  float e=expf(dt*lr); float ar=e*cosf(dt*li), ai=e*sinf(dt*li);
  float nr=ar-1.f, ni=ai; float den=lr*lr+li*li;
  float cr=(nr*lr+ni*li)/den, ci=(ni*lr-nr*li)/den;
  float hr=0,hi=0;
  for(int t=0;t<L;t++){
    float a=bu[(t*P+p)*2], b=bu[(t*P+p)*2+1];
    float ur=cr*a-ci*b, ui=cr*b+ci*a;
    float nhr=ar*hr-ai*hi+ur, nhi=ar*hi+ai*hr+ui; hr=nhr; hi=nhi;
    bu[(t*P+p)*2]=hr; bu[(t*P+p)*2+1]=hi;
  }
}
__global__ void k_ffn1(const u16* xn,const u16* w1,const u16* b1,u16* h){
  int f=blockIdx.x*64+threadIdx.x; int t=blockIdx.y;
  float a=0;
  for(int d=0;d<D;d++) a+=bf2f(xn[t*D+d])*bf2f(w1[d*F+f]);
  a=a+bf2f(b1[f]);
  float u=0.7978845608028654f*(a+0.044715f*a*a*a);
  h[t*F+f]=f2bf(0.5f*a*(1.f+tanhf(u)));
}
__global__ void k_out(const u16* x,const u16* xn,const u16* h,const u16* w2,const u16* b2,const float* hs,const u16* Cre,const u16* Cim,const u16* Dsk,float* out){
  int d=blockIdx.x*64+threadIdx.x; int t=blockIdx.y;
  float a=0;
  for(int f=0;f<F;f++) a+=bf2f(h[t*F+f])*bf2f(w2[f*D+d]);
  a=a+bf2f(b2[d]);
  float g=a/(1.f+expf(-a));
  float y=0;
  for(int p=0;p<P;p++) y+=hs[(t*P+p)*2]*bf2f(Cre[d*P+p])-hs[(t*P+p)*2+1]*bf2f(Cim[d*P+p]);
  float s=2.f*y+bf2f(xn[t*D+d])*bf2f(Dsk[d]);
  out[t*D+d]=bf2f(x[t*D+d])+s*g;
}

extern "C" void kernel_launch(void* const* d_in, const int* in_sizes, int n_in,
                              void* d_out, int out_size, void* d_ws, size_t ws_size,
                              hipStream_t stream) {
  const u16* x=(const u16*)d_in[0]; const u16* Lre=(const u16*)d_in[1]; const u16* Lim=(const u16*)d_in[2];
  const u16* Bre=(const u16*)d_in[3]; const u16* Bim=(const u16*)d_in[4]; const u16* Cre=(const u16*)d_in[5]; const u16* Cim=(const u16*)d_in[6];
  const u16* ldt=(const u16*)d_in[7]; const u16* Dsk=(const u16*)d_in[8];
  const u16* w1=(const u16*)d_in[10]; const u16* b1=(const u16*)d_in[11]; const u16* w2=(const u16*)d_in[12]; const u16* b2=(const u16*)d_in[13];
  char* ws=(char*)d_ws;
  u16* xn=(u16*)ws; float* bu=(float*)(ws+(4<<20)); u16* h=(u16*)(ws+(32<<20));
  k_ln<<<L,64,0,stream>>>(x,xn);
  k_bu<<<dim3(P/64,L),64,0,stream>>>(xn,Bre,Bim,bu);
  k_scan<<<P/64,64,0,stream>>>(Lre,Lim,ldt,bu);
  k_ffn1<<<dim3(F/64,L),64,0,stream>>>(xn,w1,b1,h);
  k_out<<<dim3(D/64,L),64,0,stream>>>(x,xn,h,w2,b2,bu,Cre,Cim,Dsk,(float*)d_out);
}
```

```cpp
#include <hip/hip_runtime.h>
#include <stdint.h>
#include <type_traits>

typedef unsigned short u16;
typedef short bf16x8 __attribute__((ext_vector_type(8)));
typedef short s16x4 __attribute__((ext_vector_type(4)));
typedef float f32x16 __attribute__((ext_vector_type(16)));
typedef float f32x4 __attribute__((ext_vector_type(4)));
typedef float f32x2 __attribute__((ext_vector_type(2)));
typedef unsigned u32x4 __attribute__((ext_vector_type(4)));
typedef unsigned u32x2 __attribute__((ext_vector_type(2)));
typedef __bf16 bf2 __attribute__((ext_vector_type(2)));
#define LAS __attribute__((address_space(3)))
#define GAS __attribute__((address_space(1)))
#define DI __device__ __forceinline__
typedef GAS unsigned gu32;
typedef GAS unsigned long long gu64;
#define RLX_AGENT __ATOMIC_RELAXED, __HIP_MEMORY_SCOPE_AGENT
template <int V> using ic = std::integral_constant<int, V>;

constexpr int L = 2048, D = 576, F = 4608;
constexpr int NFFN = 12, NSL = 16, FS = 384, NCH = 5, NSIG = 32 * NCH;
constexpr int LDS_BYTES = 163840;
constexpr size_t PART_BLK = 9 * 8 * 2048;
constexpr size_t WS_PART = 0;
constexpr size_t WS_STAT = WS_PART + (size_t)NSL * 16 * PART_BLK;
constexpr size_t WS_EPOCH = WS_STAT + L * 8;
constexpr size_t WS_GRAN = WS_EPOCH + 1024;
constexpr size_t WS_DBG = WS_GRAN + 4 * 16 * 2 * NSIG * 8;
constexpr float BETA = 0.46866869f;
constexpr float KAP = 4.9122295f;
constexpr float IBETA = 1.0f / BETA;

#define SBAR() __builtin_amdgcn_sched_barrier(0)
#define PIN(x) asm volatile("" : "+v"(x))
#define MFMA(a, b, c) __builtin_amdgcn_mfma_f32_32x32x16_bf16(a, b, c, 0, 0, 0)

DI float lo2f(unsigned v) { return __uint_as_float(v << 16); }
DI float hi2f(unsigned v) { return __uint_as_float(v & 0xffff0000u); }
DI unsigned pk2(float a, float b) { bf2 r = {(__bf16)a, (__bf16)b}; return __builtin_bit_cast(unsigned, r); }
DI float rbf(float a) { return (float)(__bf16)a; }
DI void dma1(unsigned voff, const char* sb, unsigned dst) {
  asm volatile("s_mov_b32 m0, %2\n\ts_nop 0\n\tglobal_load_lds_dwordx4 %0, %1" :: "v"(voff), "s"(sb), "s"(dst) : "memory");
}
DI void dma1v(const char* src, unsigned dst) {
  asm volatile("s_mov_b32 m0, %1\n\ts_nop 0\n\tglobal_load_lds_dwordx4 %0, off" :: "v"(src), "s"(dst) : "memory");
}
template <int I, int N, class Fn> DI void sfor(Fn&& f) { if constexpr (I < N) { f(ic<I>{}); sfor<I + 1, N>(f); } }
template <int V, int G> DI void wait_bar() { SBAR(); asm volatile("s_waitcnt vmcnt(%0) lgkmcnt(%1)\n\ts_barrier" :: "n"(V), "n"(G) : "memory"); SBAR(); }
DI s16x4 trd(unsigned a) { return __builtin_amdgcn_ds_read_tr16_b64_v4i16((LAS s16x4*)a); }
DI bf16x8 cat(s16x4 lo, s16x4 hi) { return __builtin_shufflevector(lo, hi, 0, 1, 2, 3, 4, 5, 6, 7); }
DI u32x4 lds128(unsigned a) { return *(LAS u32x4*)a; }
struct cf { float r, i; };
DI cf cmul(cf a, cf b) { return {__builtin_fmaf(a.r, b.r, -a.i * b.i), __builtin_fmaf(a.r, b.i, a.i * b.r)}; }
DI cf csq(cf a) { return {__builtin_fmaf(a.r, a.r, -a.i * a.i), (a.r + a.r) * a.i}; }
DI cf cmad(cf a, cf b, cf c) { return {__builtin_fmaf(a.r, b.r, __builtin_fmaf(-a.i, b.i, c.r)), __builtin_fmaf(a.r, b.i, __builtin_fmaf(a.i, b.r, c.i))}; }
DI void swap32(float x, float& lo, float& hi) { auto r = __builtin_amdgcn_permlane32_swap(__float_as_uint(x), __float_as_uint(x), false, false); lo = __uint_as_float(r[0]); hi = __uint_as_float(r[1]); }
DI void swap16(float x, float y, float& a, float& b) { auto r = __builtin_amdgcn_permlane16_swap(__float_as_uint(x), __float_as_uint(y), false, false); a = __uint_as_float(r[0]); b = __uint_as_float(r[1]); }

constexpr int MAXI = 6, LOOK = 18;
template <bool SSM> struct Cfg {
  static constexpr int NST = SSM ? 6 * NCH : 36, NP = 3 * NST, NP1 = NP / 2, PITCH = SSM ? 8448 : 8192, NSLOT = SSM ? 18 : 19;
  static constexpr int UNIT2 = SSM ? 4 * NCH : 24;
  static constexpr int MISC = NSLOT * PITCH;
};
struct Sched { int slot[108]; int first[41]; int vm[36]; };
constexpr int pslot(int ch) { return ch == 0 ? 17 : ch == 1 ? 15 : ch == 2 ? 13 : ch == 3 ? 11 : ch == 4 ? 17 : 15; }
constexpr int HSLOT = 14;
constexpr int dstep(int k, int cnt) { return cnt <= 3 ? 4 * k + 3 : 2 * k + 1; }
template <bool SSM> constexpr bool store_at(int st, int n) {
  using C = Cfg<SSM>; const int m = (st - C::NST / 2) * 12 + n; return m >= C::UNIT2 && (m % C::UNIT2 == 4 || m % C::UNIT2 == 8); }
template <bool SSM> constexpr Sched make_sched() {
  using C = Cfg<SSM>;
  Sched s{};
  int free_at[19] = {}, b0[19] = {}, b1[19] = {};
  for (int i = 0; i < 19; ++i) { free_at[i] = i < 6 ? -2 : i < 12 ? -1 : i < 18 ? 0 : -4; b0[i] = 100; b1[i] = 100; }
  if (SSM) { for (int i = HSLOT; i < HSLOT + 4; ++i) { b0[i] = C::NST / 2; b1[i] = C::NST / 2; } }
  else { b0[17] = b0[18] = 5; b1[17] = b1[18] = 19; b0[15] = b0[16] = 8; b1[15] = b1[16] = 20; b0[13] = b0[14] = 11; b1[13] = b1[14] = 17; b0[11] = b0[12] = 14; b1[11] = b1[12] = 18; }
  int p = 0;
  for (int st = -4; st < C::NST; ++st) {
    s.first[st + 4] = p;
    for (int k = 0; k < MAXI && p < C::NP && p < 3 * (st < 0 ? 0 : st) + LOOK; ++k) {
      const int end = p / 3 + 1; int best = -1;
      for (int i = C::NSLOT - 1; i >= 0; --i) if (free_at[i] <= st && (end <= b0[i] || st >= b1[i])) { best = i; break; }
      if (best < 0) break;
      s.slot[p] = best; free_at[best] = end; ++p;
    }
  }
  s.first[C::NST + 4] = p;
  for (int g = 0; g < C::NST; ++g) {
    int need = 3 * g + 3; if (need > C::NP - 1) need = C::NP - 1;
    int cnt = 0; bool seen = false;
    for (int st = -4; st < g; ++st) {
      const int f = s.first[st + 4], n = s.first[st + 5] - f;
      for (int step = 0; step < 12; ++step) {
        if (seen && store_at<SSM>(st, step)) ++cnt;
        for (int k = 0; k < n; ++k) if (dstep(k, n) == step) { if (seen) ++cnt; if (f + k == need) seen = true; }
      }
    }
    s.vm[g] = (seen && p == C::NP) ? cnt : -1;
  }
  return s;
}
template <bool SSM> struct SC { static constexpr Sched s = make_sched<SSM>(); };
template <bool SSM> constexpr bool sched_ok() { for (int g = 0; g < Cfg<SSM>::NST; ++g) if (SC<SSM>::s.vm[g] < 0 || SC<SSM>::s.vm[g] > 63) return false; return true; }
static_assert(sched_ok<false>() && sched_ok<true>(), "bad schedule");

#define TS(i)

struct Args {
  const u16 *x, *lre, *lim, *bre, *bim, *cre, *cim, *ldt, *dsk, *w1, *b1, *w2, *b2;
  char* ws; float* out;
};

template <bool SSM> DI void body(const Args& a, const unsigned lds0, const int slice, const int rb) {
  using C = Cfg<SSM>;
#define S SC<SSM>::s
  constexpr int PITCH = C::PITCH, STAT = C::MISC, TAB = STAT + 2048, SEG = TAB + NSIG * 16;
  int tid = threadIdx.x, lane = tid & 63;
  const int w = __builtin_amdgcn_readfirstlane(tid >> 6), r = w & 3, c = w >> 2;
  TS(0);
  const int f0 = slice * FS, t0 = rb * 128;
  const int sg = slice - NFFN, p0 = sg < 2 ? sg * 160 : 160 + sg * 64 + (sg - 2) * 64;
  int h = lane >> 5, l31 = lane & 31;
  const int drow = 8 * w + (lane >> 3), dcp = lane & 7;
  const unsigned voffX = drow * 1152 + ((dcp ^ ((drow >> 1) & 7)) << 4);
  const char* xb = (const char*)(a.x + (size_t)t0 * D);
  auto xissue = [&](int g) {
#pragma unroll
    for (int xp = 6 * g; xp < 6 * g + 6; ++xp) dma1(voffX, xb + (xp >> 1) * 128 + (xp & 1) * (64 * 1152), lds0 + w * 1024 + xp * PITCH); };
  u32x2 braw; unsigned plr, pli, pdt, tag;
  if constexpr (SSM) {
    const int sp = p0 + (tid < NSIG ? tid : NSIG - 1);
    asm volatile("global_load_ushort %0, %1, off" : "=v"(plr) : "v"(a.lre + sp) : "memory");
    asm volatile("global_load_ushort %0, %1, off" : "=v"(pli) : "v"(a.lim + sp) : "memory");
    asm volatile("global_load_ushort %0, %1, off" : "=v"(pdt) : "v"(a.ldt + sp) : "memory");
    asm volatile("global_load_dword %0, %1, off sc1" : "=v"(tag) : "v"(a.ws + WS_EPOCH + sg * 256) : "memory");
  } else {
    asm volatile("global_load_dwordx2 %0, %1, off" : "=v"(braw) : "v"(a.b1 + f0 + 4 * (tid < 96 ? tid : 95)) : "memory");
  }
  xissue(0); xissue(1);
  const unsigned ddst = lds0 + w * (SSM ? 1056 : 1024);
  unsigned voff1, voff2; const char *sb1, *sb2;
  if constexpr (SSM) {
    const int sw = dcp ^ ((drow >> 1) & 1);
    voff1 = (16 * c + 8 * (w & 1) + (lane >> 3)) * 1152 + (sw << 4);
    sb1 = (const char*)(((w >> 1) & 1) ? a.bim : a.bre) + (size_t)p0 * 1152;
  } else {
    const unsigned sw = (dcp ^ (((drow >> 1) & 1) << 2)) << 4;
    voff1 = drow * 9216 + sw; voff2 = drow * 1152 + sw;
    sb1 = (const char*)(a.w1 + f0); sb2 = (const char*)(a.w2 + (size_t)f0 * D);
  }
  const char* csrc;
  auto issue = [&](auto p_) {
    constexpr int p = p_, q = p - C::NP1;
    constexpr int so = S.slot[p] * PITCH;
    const unsigned dst = ddst + so;
    if constexpr (SSM) {
      if constexpr (p < C::NP1) dma1(voff1, sb1 + (p / 9) * (32 * 1152) + (p % 9) * 128, dst);
      else {
        if constexpr (q == 0) { int tz = threadIdx.x; PIN(tz); const int dr = tz >> 3, sw = (tz & 7) ^ ((dr >> 1) & 1);
          csrc = (const char*)((sw & 4) ? a.cim : a.cre) + dr * 1152 + p0 * 2 + (sw & 3) * 16; }
        dma1v(csrc + (q / NCH) * (64 * 1152) + (q % NCH) * 64, dst);
      }
    } else {
      if constexpr (p < C::NP1) dma1(voff1, sb1 + (p / 9) * 128 + (size_t)(p % 9) * (64 * 9216), dst);
      else dma1(voff2, sb2 + (q / 6) * 128 + (q % 6) * (64 * 1152), dst);
    }
  };
  u32x4 xf[36];
  const int xrow = 32 * (r & 1) + l31, xkey = (xrow >> 1) & 7;
  const unsigned xbase = lds0 + (r >> 1) * PITCH + xrow * 128;
  auto xread = [&](int g) {
#pragma unroll
    for (int kk = 0; kk < 4; ++kk) { unsigned b = xbase + (((2 * kk + h) ^ xkey) << 4) + g * 6 * PITCH; PIN(b);
#pragma unroll
      for (int i = 0; i < 3; ++i) xf[(3 * g + i) * 4 + kk] = lds128(b + i * 2 * PITCH); } };
  float sa[4] = {}, sq[4] = {};
  auto xstat1 = [&](int g, int cc) {
    const bf2 one = {(__bf16)1.0f, (__bf16)1.0f};
#pragma unroll
    for (int i = 3 * g; i < 3 * g + 3; ++i)
#pragma unroll
      for (int k2 = 0; k2 < 2; ++k2)
#pragma unroll
        for (int e = 0; e < 4; ++e) { const unsigned ve = xf[4 * i + 2 * cc + k2][e];
          bf2 p = __builtin_bit_cast(bf2, ve); sa[e] = __builtin_amdgcn_fdot2_f32_bf16(p, one, sa[e], false); sq[e] = __builtin_amdgcn_fdot2_f32_bf16(p, p, sq[e], false); } };
  auto xstat = [&](int g) { if (c) { asm volatile("; hi"); xstat1(g, 1); asm volatile("; hi end"); } else { asm volatile("; lo"); xstat1(g, 0); asm volatile("; lo end"); } };
  wait_bar<6, 0>();
  TS(1);
  xread(0);
  xissue(2);
  sfor<S.first[0], S.first[2]>(issue);
  if constexpr (SSM) {
    PIN(plr); PIN(pli); PIN(pdt); PIN(tag);
    tag += 1u;
    if (tid < NSIG) {
      const float lr = lo2f(plr), li = lo2f(pli), dt = rbf(expf(lo2f(pdt)));
      const float e = expf(dt * lr); float sn, cs; sincosf(dt * li, &sn, &cs);
      const float ar = e * cs, ai = e * sn, nr = ar - 1.f, den = lr * lr + li * li;
      const float k2 = (sg >= 2 && tid < 32) ? 0.f : 2.f / den;
      *(LAS f32x4*)(lds0 + TAB + tid * 16) = (f32x4){ar, ai, (nr * lr + ai * li) * k2, (ai * lr - nr * li) * k2};
    }
    tag = __builtin_amdgcn_readfirstlane(tag);
  } else {
    PIN(braw);
    if (tid < 96) *(LAS f32x4*)(lds0 + TAB + tid * 16) = (f32x4){BETA * lo2f(braw[0]), BETA * hi2f(braw[0]), BETA * lo2f(braw[1]), BETA * hi2f(braw[1])};
  }
  xstat(0);
  wait_bar<6 + S.first[2], 0>();
  xread(1);
  sfor<S.first[2], S.first[3]>(issue);
  xstat(1);
  wait_bar<S.first[3], 0>();
  TS(2);
  xread(2);
  sfor<S.first[3], S.first[4]>(issue);
  xstat(2);
  float s1 = (sa[0] + sa[1]) + (sa[2] + sa[3]), s2 = (sq[0] + sq[1]) + (sq[2] + sq[3]);
  { float u, v; swap32(s1, u, v); s1 = u + v; swap32(s2, u, v); s2 = u + v; }
  if (h == 0) *(LAS f32x2*)(lds0 + STAT + (w * 32 + l31) * 8) = (f32x2){s1, s2};
  unsigned fb1[3], fb2[3];
  if constexpr (SSM) {
    const int row = 32 * c + l31;
#pragma unroll
    for (int i = 0; i < 3; ++i) { fb1[i] = lds0 + (row >> 3) * 1056 + (row & 7) * 128 + ((h ^ ((row >> 1) & 1)) << 4) + i * 65536; }
  } else {
    const int q4 = (lane & 15) >> 2, p4 = lane & 3, cb = (lane >> 4) & 1;
    const unsigned colb = (((c * 4 + cb * 2 + (p4 >> 1)) ^ ((q4 >> 1) << 2)) << 4) + 8 * (p4 & 1);
#pragma unroll
    for (int i = 0; i < 3; ++i) { fb1[i] = lds0 + (8 * h + q4) * 128 + colb + i * 65536; fb2[i] = lds0 + (4 * h + q4) * 128 + colb + i * 65536; }
  }
  PIN(fb1[1]); PIN(fb1[2]);
  auto fread = [&](auto m_) -> bf16x8 {
    constexpr int p = m_ / 4, kk = m_ & 3, off = S.slot[p] * PITCH + kk * (SSM ? 32 : 2048);
    if constexpr (SSM) return __builtin_bit_cast(bf16x8, lds128(fb1[off >> 16] + (off & 65535)));
    else if constexpr (p < C::NP1) return cat(trd(fb1[off >> 16] + (off & 65535)), trd(fb1[off >> 16] + (off & 65535) + 512));
    else return cat(trd(fb2[off >> 16] + (off & 65535)), trd(fb2[off >> 16] + (off & 65535) + 1024)); };
  unsigned pwr, prd, biasb;
  if constexpr (!SSM) { pwr = lds0 + w * 2048 + lane * 16 + 11 * PITCH; prd = lds0 + r * 2048 + lane * 16 + 11 * PITCH; PIN(pwr);
    biasb = lds0 + TAB + (32 * c + 4 * h) * 4; PIN(biasb); }
  wait_bar<S.vm[0], 0>();
  TS(3);
  bf16x8 af[4];
  af[0] = fread(ic<0>{}); af[1] = fread(ic<1>{}); af[2] = fread(ic<2>{});
  f32x16 acc, prev;
  if constexpr (!SSM) {
#pragma unroll
    for (int g = 0; g < 4; ++g) { f32x4 b = *(LAS f32x4*)(biasb + g * 32); acc[4 * g] = b[0]; acc[4 * g + 1] = b[1]; acc[4 * g + 2] = b[2]; acc[4 * g + 3] = b[3]; } }
  float inv, nb;
  { f32x2 o = *(LAS f32x2*)(lds0 + STAT + ((w ^ 4) * 32 + l31) * 8);
    s1 += o[0]; s2 += o[1];
    const float mean = s1 * (1.f / D);
    const float var = fmaxf(s2 * (1.f / D) - mean * mean, 0.f);
    const float mb = rbf(mean), den = rbf(sqrtf(rbf(rbf(var) + 1e-5f)));
    inv = (SSM ? 1.f : BETA) / den; nb = -mb * inv;
    if constexpr (SSM) { if (sg == 3 && c == 0 && h == 0) *(f32x2*)(a.ws + WS_STAT + (size_t)(t0 + 32 * r + l31) * 8) = (f32x2){mb, den}; }
  }
  auto xnorm = [&](int n) {
    float t[8];
#pragma unroll
    for (int e = 0; e < 4; ++e) { t[2 * e] = lo2f(xf[n][e]); t[2 * e + 1] = hi2f(xf[n][e]); }
    asm volatile("" : "+v"(t[0]), "+v"(t[1]), "+v"(t[2]), "+v"(t[3]), "+v"(t[4]), "+v"(t[5]), "+v"(t[6]), "+v"(t[7]));
#pragma unroll
    for (int e = 0; e < 8; ++e) t[e] = __builtin_fmaf(t[e], inv, nb);
    asm volatile("" : "+v"(t[0]), "+v"(t[1]), "+v"(t[2]), "+v"(t[3]), "+v"(t[4]), "+v"(t[5]), "+v"(t[6]), "+v"(t[7]));
#pragma unroll
    for (int e = 0; e < 4; ++e) xf[n][e] = pk2(t[2 * e], t[2 * e + 1]);
    PIN(xf[n]); };
  xnorm(0);
  u32x4 P[6][4], pn[2], nbias[4];
  f32x16 raw[NCH];
  const f32x16 zero16 = {};
  float gt0, gt1;
  char* pbase;
  auto gelu_ph = [&](int e, int ph) {
    const float v0 = prev[2 * e], v1 = prev[2 * e + 1];
    if (ph == 0) { gt0 = v0 * __builtin_fmaf(v0, v0, KAP); gt1 = v1 * __builtin_fmaf(v1, v1, KAP); PIN(gt0); PIN(gt1); }
    if (ph == 1) { gt0 = __builtin_amdgcn_exp2f(-gt0); gt1 = __builtin_amdgcn_exp2f(-gt1); PIN(gt0); PIN(gt1); }
    if (ph == 2) { gt0 = __builtin_amdgcn_rcpf(1.f + gt0); gt1 = 1.f + gt1; PIN(gt0); PIN(gt1); }
    if (ph == 3) { unsigned pv = pk2(v0 * gt0, v1 * __builtin_amdgcn_rcpf(gt1)); PIN(pv); pn[e >> 2][e & 3] = pv; }
  };
  auto ssm_scan = [&]() {
    tid = threadIdx.x; PIN(tid); lane = tid & 63; h = lane >> 5;
    const int g = (lane >> 4) & 1, s16 = lane & 15;
    cf u[NCH][8], Elo[NCH][2], U0[NCH], V0[NCH];
#pragma unroll
    for (int ch = 0; ch < NCH; ++ch) {
      const f32x4 t = *(LAS f32x4*)(lds0 + TAB + (32 * ch + 16 * c + s16) * 16);
      const cf av = {t[0], t[1]}, a4 = csq(csq(av)), a8 = csq(a4), a16 = csq(a8);
#pragma unroll
      for (int j = 0; j < 8; ++j) swap16(raw[ch][j], raw[ch][j + 8], u[ch][j].r, u[ch][j].i);
      cf Ub[2];
#pragma unroll
      for (int b = 0; b < 2; ++b) {
        const cf e = cmad(av, cmad(av, cmad(av, u[ch][4 * b], u[ch][4 * b + 1]), u[ch][4 * b + 2]), u[ch][4 * b + 3]);
        cf ehi; swap32(e.r, Elo[ch][b].r, ehi.r); swap32(e.i, Elo[ch][b].i, ehi.i);
        Ub[b] = cmad(a4, Elo[ch][b], ehi);
      }
      U0[ch] = Ub[0];
      const cf V = cmad(a8, Ub[0], Ub[1]);
      cf V1; swap16(V.r, V.r, V0[ch].r, V1.r); swap16(V.i, V.i, V0[ch].i, V1.i);
      const cf Sg = cmad(a16, V0[ch], V1);
      if (lane < 16) *(LAS f32x2*)(lds0 + SEG + (r * NSIG + 32 * ch + 16 * c + s16) * 8) = (f32x2){Sg.r, Sg.i};
    }
    wait_bar<63, 0>();
    TS(8);
    if (tid < NSIG) {
      const f32x4 t = *(LAS f32x4*)(lds0 + TAB + tid * 16);
      const cf a32 = csq(csq(csq(csq(csq(cf{t[0], t[1]}))))), a128 = csq(csq(a32));
      cf sv[4];
#pragma unroll
      for (int i = 0; i < 4; ++i) { const f32x2 v = *(LAS f32x2*)(lds0 + SEG + (i * NSIG + tid) * 8); sv[i] = {v[0], v[1]}; }
      const auto rs = __builtin_amdgcn_make_buffer_rsrc(a.ws + WS_GRAN + (size_t)sg * (16 * NSIG * 16), 0, 16 * NSIG * 16, 0x00020000);
      const cf T = cmad(a32, cmad(a32, cmad(a32, sv[0], sv[1]), sv[2]), sv[3]);
      __builtin_amdgcn_raw_buffer_store_b128((u32x4){__float_as_uint(T.r), tag, __float_as_uint(T.i), tag}, rs, tid * 16, rb * (NSIG * 16), 16);
      cf G;
      for (unsigned spins = 0; spins < (1u << 16); ++spins) {
        u32x4 v[15]; bool ok = true;
#pragma unroll
        for (int k = 0; k < 15; ++k) { const int j = rb - 15 + k; v[k] = __builtin_amdgcn_raw_buffer_load_b128(rs, tid * 16, (j < 0 ? 0 : j) * (NSIG * 16), 16); }
        G = {0.f, 0.f};
#pragma unroll
        for (int k = 0; k < 15; ++k) { const bool use = rb - 15 + k >= 0;
          ok &= !use || (v[k][1] == tag && v[k][3] == tag);
          G = cmad(a128, G, cf{use ? __uint_as_float(v[k][0]) : 0.f, use ? __uint_as_float(v[k][2]) : 0.f}); }
        if (__all(ok)) break;
        __builtin_amdgcn_s_sleep(2);
      }
#pragma unroll
      for (int i = 0; i < 4; ++i) { *(LAS f32x2*)(lds0 + SEG + (i * NSIG + tid) * 8) = (f32x2){G.r, G.i}; G = cmad(a32, G, sv[i]); }
    }
    wait_bar<63, 0>();
    TS(9);
    if (rb == 15 && tid == 0) __hip_atomic_store((gu32*)(a.ws + WS_EPOCH + sg * 256), tag, RLX_AGENT);
    const int q4 = (lane & 15) >> 2, p4 = lane & 3;
    const unsigned hw = lds0 + HSLOT * PITCH + r * 4096 + (16 * c + s16) * 64;
    const int wkey = (s16 >> 1) & 7;
    unsigned hr[2];
#pragma unroll
    for (int uu = 0; uu < 2; ++uu) hr[uu] = lds0 + HSLOT * PITCH + r * 4096 + (8 * h + 4 * uu + q4) * 64 + (((g * 4 + p4) ^ (4 * h + 2 * uu + (q4 >> 1))) << 3);
#pragma unroll
    for (int ch = 0; ch < NCH; ++ch) {
      const f32x2 ci = *(LAS f32x2*)(lds0 + SEG + (r * NSIG + 32 * ch + 16 * c + s16) * 8);
      const f32x4 t = *(LAS f32x4*)(lds0 + TAB + (32 * ch + 16 * c + s16) * 16);
      const cf av = {t[0], t[1]}, k2 = {t[2], t[3]}, a4 = csq(csq(av)), a8 = csq(a4), a16 = csq(a8);
      const cf cin = {ci[0], ci[1]}, c16 = cmad(a16, cin, V0[ch]);
      cf kb[2]; kb[0] = g ? c16 : cin; kb[1] = cmad(a8, kb[0], U0[ch]);
#pragma unroll
      for (int b = 0; b < 2; ++b) {
        const cf c4 = cmad(a4, kb[b], Elo[ch][b]);
        cf z = h ? c4 : kb[b];
        float hre[4], him[4];
#pragma unroll
        for (int i = 0; i < 4; ++i) { z = cmad(av, z, u[ch][4 * b + i]); const cf o = cmul(k2, z); hre[i] = o.r; him[i] = -o.i; }
        const unsigned ad = hw + (ch & 1) * 16384 + (((4 * g + 2 * b + h) ^ wkey) << 3);
        *(LAS u32x2*)(ad) = (u32x2){pk2(hre[0], hre[1]), pk2(hre[2], hre[3])};
        *(LAS u32x2*)(ad + 2048) = (u32x2){pk2(him[0], him[1]), pk2(him[2], him[3])};
      }
      wait_bar<63, 0>();
#pragma unroll
      for (int kk = 0; kk < 4; ++kk) P[ch][kk] = __builtin_bit_cast(u32x4, cat(trd(hr[0] + (ch & 1) * 16384 + kk * 1024), trd(hr[1] + (ch & 1) * 16384 + kk * 1024)));
    }
  };
  sfor<0, C::NST>([&](auto gs_) {
    constexpr int gs = gs_;
    constexpr bool ph1 = gs < C::NST / 2;
    constexpr int f = S.first[gs + 4], cnt = S.first[gs + 5] - f;
    if constexpr (gs == 3) { TS(4); }
    if constexpr (gs == C::NST / 2) { TS(5);
      if constexpr (SSM) ssm_scan();
      int tz = threadIdx.x; PIN(tz);
      pbase = a.ws + WS_PART + (size_t)(slice * 16 + rb) * PART_BLK + w * 2048 + (tz & 63) * 16; }
    if constexpr (!SSM && gs == 15) { PIN(fb2[0]); PIN(fb2[1]); PIN(fb2[2]); PIN(prd); }
    constexpr bool lgk0 = SSM ? gs == C::NST / 2 : ((ph1 && gs % 3 == 0 && gs >= 6) || (gs >= 17 && gs <= 20));
    if constexpr (gs > 0) wait_bar<S.vm[gs], (lgk0 ? 0 : 15)>();
    sfor<0, 12>([&](auto n_) {
      constexpr int n = n_, m = 12 * gs + n;
      constexpr int un = ph1 ? m / 36 : (m - 6 * C::NST) / C::UNIT2, gn = ph1 ? m % 36 : (m - 6 * C::NST) % C::UNIT2;
      if constexpr (m + 3 < 12 * C::NST) af[(m + 3) & 3] = fread(ic<m + 3>{});
      if constexpr (!SSM) {
        if constexpr (gs == 16) P[n >> 2][n & 3] = lds128(prd + (pslot(n >> 2) - 11) * PITCH + ((n & 3) >> 1) * PITCH + (n & 1) * 1024);
        if constexpr (gs >= 17 && gs <= 19 && n < 4) P[gs - 14][n] = lds128(prd + (pslot(gs - 14) - 11) * PITCH + (n >> 1) * PITCH + (n & 1) * 1024);
        if constexpr (ph1 && un < 5 && gn >= 30 && gn < 34) nbias[gn - 30] = lds128(biasb + (un + 1) * 256 + (gn - 30) * 32);
      }
      if constexpr (ph1) {
        if constexpr (SSM) acc = MFMA(__builtin_bit_cast(bf16x8, xf[gn]), af[n & 3], gn == 0 ? zero16 : acc);
        else acc = MFMA(af[n & 3], __builtin_bit_cast(bf16x8, xf[gn]), acc);
      } else acc = MFMA(af[n & 3], __builtin_bit_cast(bf16x8, P[gn >> 2][gn & 3]), gn == 0 ? zero16 : acc);
      SBAR();
      if constexpr (m + 1 < 36) xnorm(m + 1);
      if constexpr (!SSM) {
        if constexpr (ph1 && un > 0 && gn >= 2 && gn < 34) gelu_ph((gn - 2) >> 2, (gn - 2) & 3);
        if constexpr (ph1 && un > 0 && gn >= 34) *(LAS u32x4*)(pwr + (pslot(un - 1) - 11) * PITCH + (gn - 34) * 1024) = pn[gn - 34];
        if constexpr (gs == 18) {
          if constexpr (n >= 1) { constexpr int k0 = (n - 1) * 3; sfor<k0, (k0 + 3 < 32 ? k0 + 3 : 32)>([&](auto k_) { constexpr int k = k_; gelu_ph(k >> 2, k & 3); }); }
          if constexpr (n == 7) *(LAS u32x4*)(pwr + (pslot(5) - 11) * PITCH) = pn[0];
          if constexpr (n == 11) *(LAS u32x4*)(pwr + (pslot(5) - 11) * PITCH + 1024) = pn[1];
        }
      }
      if constexpr (store_at<SSM>(gs, n)) {
        constexpr int e = gn == 4 ? 0 : 8;
        u32x4 o = {pk2(prev[e], prev[e + 1]), pk2(prev[e + 2], prev[e + 3]), pk2(prev[e + 4], prev[e + 5]), pk2(prev[e + 6], prev[e + 7])};
        *(u32x4*)(pbase + (un - 1) * 16384 + (e >> 3) * 1024) = o;
      }
      sfor<0, cnt>([&](auto k_) { constexpr int k = k_; if constexpr (dstep(k, cnt) == n) issue(ic<f + k>{}); });
      if constexpr (gn == (ph1 ? 36 : C::UNIT2) - 1) {
        if constexpr (SSM && ph1) raw[un] = acc; else prev = acc;
        if constexpr (!SSM && ph1 && un < 5) {
#pragma unroll
          for (int g = 0; g < 4; ++g)
#pragma unroll
            for (int e = 0; e < 4; ++e) acc[4 * g + e] = __uint_as_float(nbias[g][e]);
        }
      }
      SBAR();
    });
  });
  TS(6);
#pragma unroll
  for (int hf = 0; hf < 2; ++hf) { const int e = hf * 8;
    u32x4 o = {pk2(prev[e], prev[e + 1]), pk2(prev[e + 2], prev[e + 3]), pk2(prev[e + 4], prev[e + 5]), pk2(prev[e + 6], prev[e + 7])};
    *(u32x4*)(pbase + 8 * 16384 + hf * 1024) = o; }
  TS(7);
}

#undef S
__global__ __launch_bounds__(512, 2) void k_main(Args a) {
  __shared__ __attribute__((aligned(16))) char smem[LDS_BYTES];
  const unsigned lds0 = (unsigned)(uintptr_t)smem;
  const int bid = blockIdx.x, xcd = bid & 7, loc = bid >> 3;
  const int slice = (loc & 3) * 4 + (xcd & 3), rb = (xcd >> 2) * 8 + (loc >> 2);
  if (slice < NFFN) body<false>(a, lds0, slice, rb); else body<true>(a, lds0, slice, rb);
}

__global__ __launch_bounds__(256) void k_fin(Args a) {
  const int gt = blockIdx.x * 256 + threadIdx.x;
  const int lane = gt & 63, st = (gt >> 6) & 1, w = (gt >> 7) & 7, rest = gt >> 10, dp = rest % 9, rb = rest / 9;
  const char* pb = a.ws + WS_PART + (size_t)rb * PART_BLK + (size_t)(gt - rb * 9216) * 16;
  u32x4 v[NSL];
#pragma unroll
  for (int s = 0; s < NSL; ++s) v[s] = *(const u32x4*)(pb + (size_t)s * 16 * PART_BLK);
  const int t = rb * 128 + (w & 3) * 32 + (lane & 31), d0 = dp * 64 + (w >> 2) * 32 + st * 16 + 4 * (lane >> 5);
  const f32x2 stt = *(const f32x2*)(a.ws + WS_STAT + (size_t)t * 8);
  u32x2 xr[2], br[2], dr[2];
#pragma unroll
  for (int g = 0; g < 2; ++g) { const int d = d0 + 8 * g; xr[g] = *(const u32x2*)(a.x + (size_t)t * D + d); br[g] = *(const u32x2*)(a.b2 + d); dr[g] = *(const u32x2*)(a.dsk + d); }
  float f[8] = {}, y[8] = {};
#pragma unroll
  for (int s = 0; s < NSL; ++s)
#pragma unroll
    for (int k = 0; k < 4; ++k) { if (s < NFFN) { f[2 * k] += lo2f(v[s][k]); f[2 * k + 1] += hi2f(v[s][k]); } else { y[2 * k] += lo2f(v[s][k]); y[2 * k + 1] += hi2f(v[s][k]); } }
  const float inv = 1.f / stt[1];
#pragma unroll
  for (int g = 0; g < 2; ++g) {
    f32x4 o;
#pragma unroll
    for (int k = 0; k < 4; ++k) {
      const float xv = (k & 1) ? hi2f(xr[g][k >> 1]) : lo2f(xr[g][k >> 1]);
      const float bv = (k & 1) ? hi2f(br[g][k >> 1]) : lo2f(br[g][k >> 1]);
      const float dv = (k & 1) ? hi2f(dr[g][k >> 1]) : lo2f(dr[g][k >> 1]);
      const float u = rbf(rbf(f[4 * g + k] * IBETA) + bv);
      const float gg = rbf(u * __builtin_amdgcn_rcpf(1.f + __expf(-u)));
      const float xn = rbf(rbf(xv - stt[0]) * inv);
      const float s = y[4 * g + k] + rbf(xn * dv);
      o[k] = xv + s * gg;
    }
    *(f32x4*)(a.out + (size_t)t * D + d0 + 8 * g) = o;
  }
}

extern "C" void kernel_launch(void* const* d_in, const int* in_sizes, int n_in, void* d_out, int out_size, void* d_ws, size_t ws_size, hipStream_t stream) {
  Args a;
  a.x = (const u16*)d_in[0]; a.lre = (const u16*)d_in[1]; a.lim = (const u16*)d_in[2]; a.bre = (const u16*)d_in[3]; a.bim = (const u16*)d_in[4];
  a.cre = (const u16*)d_in[5]; a.cim = (const u16*)d_in[6]; a.ldt = (const u16*)d_in[7]; a.dsk = (const u16*)d_in[8];
  a.w1 = (const u16*)d_in[10]; a.b1 = (const u16*)d_in[11]; a.w2 = (const u16*)d_in[12]; a.b2 = (const u16*)d_in[13];
  a.ws = (char*)d_ws + ((ws_size - (40u << 20)) & ~(size_t)4095); a.out = (float*)d_out;
  k_main<<<256, 512, 0, stream>>>(a);
  k_fin<<<576, 256, 0, stream>>>(a);
}
#define NSTAMP 10
#define STAMP_VALID(b) 1
#define STAMP_CLASS(b) ((((b) >> 3) & 3) == 3)
```

```cpp
#define SC1ST
#define CODEPF 28672
#include <hip/hip_runtime.h>
#include <stdint.h>
#include <type_traits>

typedef unsigned short u16;
typedef short bf16x8 __attribute__((ext_vector_type(8)));
typedef short s16x4 __attribute__((ext_vector_type(4)));
typedef float f32x16 __attribute__((ext_vector_type(16)));
typedef float f32x4 __attribute__((ext_vector_type(4)));
typedef float f32x2 __attribute__((ext_vector_type(2)));
typedef unsigned u32x4 __attribute__((ext_vector_type(4)));
typedef unsigned u32x2 __attribute__((ext_vector_type(2)));
typedef __bf16 bf2 __attribute__((ext_vector_type(2)));
#define LAS __attribute__((address_space(3)))
#define GAS __attribute__((address_space(1)))
#define DI __device__ __forceinline__
typedef GAS unsigned gu32;
typedef GAS unsigned long long gu64;
#define RLX_AGENT __ATOMIC_RELAXED, __HIP_MEMORY_SCOPE_AGENT
template <int V> using ic = std::integral_constant<int, V>;

constexpr int L = 2048, D = 576, F = 4608;
constexpr int NFFN = 12, NSL = 16, FS = 384, NCH = 5, NSIG = 32 * NCH;
constexpr int LDS_BYTES = 163840;
constexpr size_t PART_BLK = 9 * 8 * 2048;
constexpr size_t WS_PART = 0;
constexpr size_t WS_STAT = WS_PART + (size_t)NSL * 16 * PART_BLK;
constexpr size_t WS_EPOCH = WS_STAT + L * 8;
constexpr size_t WS_GRAN = WS_EPOCH + 1024;
constexpr size_t WS_REPOCH = WS_GRAN + 4 * 16 * 2 * NSIG * 8;
constexpr size_t WS_FLAG = WS_REPOCH + 16 * 256;
constexpr size_t WS_DBG = WS_FLAG + 16 * 16 * 256;
constexpr float BETA = 0.46866869f;
constexpr float KAP = 4.9122295f;
constexpr float IBETA = 1.0f / BETA;

#define SBAR() __builtin_amdgcn_sched_barrier(0)
#define PIN(x) asm volatile("" : "+v"(x))
#define MFMA(a, b, c) __builtin_amdgcn_mfma_f32_32x32x16_bf16(a, b, c, 0, 0, 0)

DI float lo2f(unsigned v) { return __uint_as_float(v << 16); }
DI float hi2f(unsigned v) { return __uint_as_float(v & 0xffff0000u); }
DI unsigned pk2(float a, float b) { bf2 r = {(__bf16)a, (__bf16)b}; return __builtin_bit_cast(unsigned, r); }
DI float rbf(float a) { return (float)(__bf16)a; }
DI void dma1(unsigned voff, const char* sb, unsigned dst) {
  asm volatile("s_mov_b32 m0, %2\n\ts_nop 0\n\tglobal_load_lds_dwordx4 %0, %1" :: "v"(voff), "s"(sb), "s"(dst) : "memory");
}
DI void dma1v(const char* src, unsigned dst) {
  asm volatile("s_mov_b32 m0, %1\n\ts_nop 0\n\tglobal_load_lds_dwordx4 %0, off" :: "v"(src), "s"(dst) : "memory");
}
template <int I, int N, class Fn> DI void sfor(Fn&& f) { if constexpr (I < N) { f(ic<I>{}); sfor<I + 1, N>(f); } }
template <int V, int G> DI void wait_bar() { SBAR(); asm volatile("s_waitcnt vmcnt(%0) lgkmcnt(%1)\n\ts_barrier" :: "n"(V), "n"(G) : "memory"); SBAR(); }
DI s16x4 trd(unsigned a) { return __builtin_amdgcn_ds_read_tr16_b64_v4i16((LAS s16x4*)a); }
DI bf16x8 cat(s16x4 lo, s16x4 hi) { return __builtin_shufflevector(lo, hi, 0, 1, 2, 3, 4, 5, 6, 7); }
DI u32x4 lds128(unsigned a) { return *(LAS u32x4*)a; }
DI void pst(unsigned voff, char* sb, u32x4 v) { asm volatile("global_store_dwordx4 %0, %1, %2 sc1\n\ts_nop 1" :: "v"(voff), "v"(v), "s"(sb) : "memory"); }
template <int OFF> DI void gload16(u32x4& d, unsigned voff, const char* sb) { asm volatile("global_load_dwordx4 %0, %1, %2 offset:%3" : "=v"(d) : "v"(voff), "s"(sb), "n"(OFF) : "memory"); }
template <int N> DI void wait6(u32x4* v) { asm volatile("s_waitcnt vmcnt(%6)" : "+v"(v[0]), "+v"(v[1]), "+v"(v[2]), "+v"(v[3]), "+v"(v[4]), "+v"(v[5]) : "n"(N) : "memory"); }
template <int N> DI void wait3(u32x4* v) { asm volatile("s_waitcnt vmcnt(%3)" : "+v"(v[0]), "+v"(v[1]), "+v"(v[2]) : "n"(N) : "memory"); }
DI void pin4(f32x2* t) { asm volatile("" : "+v"(t[0]), "+v"(t[1]), "+v"(t[2]), "+v"(t[3])); }
DI f32x4 mf4(u32x2 x, u32x2 y, f32x4 z) { return __builtin_amdgcn_mfma_f32_4x4x4bf16_1k(__builtin_bit_cast(s16x4, x), __builtin_bit_cast(s16x4, y), z, 0, 0, 0); }
DI void pin1(unsigned& v) { PIN(v); }
DI void stamp2(unsigned ad) { unsigned long long t; asm volatile("s_memtime %0\n\ts_waitcnt lgkmcnt(0)" : "=s"(t) :: "memory"); *(LAS unsigned*)ad = (unsigned)t; }
DI void dma1s(unsigned voff, const char* sb, unsigned dst) {
  asm volatile("s_mov_b32 m0, %2\n\ts_nop 0\n\tglobal_load_lds_dwordx4 %0, %1 sc1" :: "v"(voff), "s"(sb), "s"(dst) : "memory");
}
DI void gload8s(u32x2& d, unsigned voff, const char* sb) { asm volatile("global_load_dwordx2 %0, %1, %2 sc1" : "=v"(d) : "v"(voff), "s"(sb) : "memory"); }
DI void pin2(u32x2& v) { PIN(v); }
DI void flag_store(char* p, unsigned n, unsigned tag, int w) {
  u32x2 v = {n, tag}; unsigned z = 0; unsigned long long keep;
  asm volatile("s_cmp_lg_u32 %4, 0\n\ts_cbranch_scc1 1f\n\ts_mov_b64 %0, exec\n\ts_mov_b64 exec, 1\n\tglobal_store_dwordx2 %1, %2, %3 sc1\n\ts_mov_b64 exec, %0\n1:"
               : "=&s"(keep) : "v"(z), "v"(v), "s"(p), "s"(w) : "memory", "scc");
}
constexpr unsigned long long GMASK = 0xFFFF0000FFFF0000ull, HMASK = 0xFFFFFFFF00000000ull;
DI float selm(float a, float b, unsigned long long m) { float r; asm("v_cndmask_b32 %0, %1, %2, %3" : "=v"(r) : "v"(a), "v"(b), "s"(m)); return r; }
DI void touch(unsigned voff, const char* sb, unsigned dst) { asm volatile("s_mov_b32 m0, %2\n\ts_nop 0\n\tglobal_load_lds_dword %0, %1" :: "v"(voff), "s"(sb), "s"(dst) : "memory"); }
template <int OFF> DI void gload_hi(float& d, unsigned voff, const char* sb) { asm volatile("global_load_short_d16_hi %0, %1, %2 offset:%3" : "+v"(d) : "v"(voff), "s"(sb), "n"(OFF) : "memory"); }
template <int CTRL> DI float dpp(float v) { return __int_as_float(__builtin_amdgcn_update_dpp(0, __float_as_int(v), CTRL, 0xf, 0xf, true)); }
struct cf { float r, i; };
DI cf cmul(cf a, cf b) { return {__builtin_fmaf(a.r, b.r, -a.i * b.i), __builtin_fmaf(a.r, b.i, a.i * b.r)}; }
DI cf csq(cf a) { return {__builtin_fmaf(a.r, a.r, -a.i * a.i), (a.r + a.r) * a.i}; }
DI cf cmad(cf a, cf b, cf c) { return {__builtin_fmaf(a.r, b.r, __builtin_fmaf(-a.i, b.i, c.r)), __builtin_fmaf(a.r, b.i, __builtin_fmaf(a.i, b.r, c.i))}; }
DI void swap32(float x, float& lo, float& hi) { auto r = __builtin_amdgcn_permlane32_swap(__float_as_uint(x), __float_as_uint(x), false, false); lo = __uint_as_float(r[0]); hi = __uint_as_float(r[1]); }
DI void swap16(float x, float y, float& a, float& b) { auto r = __builtin_amdgcn_permlane16_swap(__float_as_uint(x), __float_as_uint(y), false, false); a = __uint_as_float(r[0]); b = __uint_as_float(r[1]); }

template <bool SSM> struct Cfg {
  static constexpr int NP = SSM ? 18 * NCH : 108, NP1 = NP / 2, NM = 4 * NP, NM1 = NM / 2, NI = NP;
  static constexpr int PITCH = SSM ? 8448 : 8192, NSLOT = SSM ? 18 : 19;
  static constexpr int UNIT2 = SSM ? 4 * NCH : 24;
  static constexpr int MISC = NSLOT * PITCH;
};
constexpr bool bar_at(int m) { return m % 12 == 0 || (m < 36 && m % 4 == 0); }
constexpr int next_bar(int m) { int b = m + 1; while (!bar_at(b)) ++b; return b; }
constexpr int item_piece(int q) { return q; }
constexpr int witem(int p) { return p; }
constexpr int pslot(int ch) { return ch == 0 ? 17 : ch == 1 ? 15 : ch == 2 ? 13 : ch == 3 ? 11 : ch == 4 ? 17 : 15; }
constexpr int HSLOT = 8, KSLOT = 14;
template <bool SSM> constexpr int p2_dp(int j) { return SSM ? j / NCH : (j < 12 ? (j & 1) : j / 6); }
template <bool SSM> constexpr int p2_ch(int j) { return SSM ? j % NCH : (j < 12 ? (j >> 1) : j % 6); }
constexpr int h_ch(int n) { return n >= 16 ? 4 : 2 * (n >> 3) + ((n >> 1) & 1); }
constexpr int h_kk(int n) { return n >= 16 ? n & 3 : 2 * ((n >> 2) & 1) + (n & 1); }
template <bool SSM> constexpr int store_of(int m) {
  using C = Cfg<SSM>; const int m2 = m - C::NM1;
  if (m2 < 0) return -1;
  if (!SSM && m2 < 72) return m2 == 50 ? 0 : m2 == 54 ? 1 : m2 == 58 ? 2 : m2 == 62 ? 3 : -1;
  if (m2 < C::UNIT2) return -1;
  return m2 % C::UNIT2 == 4 ? 2 * (m2 / C::UNIT2 - 1) : m2 % C::UNIT2 == 8 ? 2 * (m2 / C::UNIT2 - 1) + 1 : -1;
}
constexpr int NPF = 9;
constexpr int pf_of(int m) { return (m >= 48 && m < 48 + 12 * NPF && m % 12 == 0) ? (m - 48) / 12 : -1; }
constexpr int I0 = 6, LOOK = 18, NXP = 6;
constexpr int xslot(int p) { return p < NXP ? 2 * p : 2 * p - 8; }
constexpr int JORD[18] = {0, 2, 4, 6, 1, 3, 5, 7, 8, 9, 10, 11, 12, 13, 14, 15, 16, 17};
#define JV 0
constexpr int JC[10] = {0, 0, 0, 2, 2, 3, 0, 0, 0, 0}, N0[10] = {0, 0, 0, 0, 0, 4, 0, 0, 0, 0}, N1[10] = {0, 0, 0, 0, 0, 3, 0, 0, 0, 0}, FD[10] = {0, 0, 0, 0, 0, 10, 0, 0, 0, 0};
constexpr int JE0 = 5, MAXPUB = 6;
constexpr int jcount(int e) { return e >= 0 && e < 10 ? JC[e] : 0; }
constexpr int jfirst(int e) { int n = 0; for (int k = 9; k > e; --k) n += JC[k]; return n; }
constexpr int NJIN = jfirst(0);
template <bool SSM> constexpr int last_store(int dp) { for (int m = Cfg<SSM>::NM - 1; m >= 0; --m) if (store_of<SSM>(m) == 2 * dp + 1) return m; return 100000; }
template <bool SSM> constexpr int pub_count(int B) { int n = 0;
  while (n < MAXPUB && last_store<SSM>(n) + (last_store<SSM>(n) < Cfg<SSM>::NM - 12 * JE0 ? 14 : SSM ? 20 : 26) <= B) ++n; return n; }
template <bool SSM> constexpr bool pub_at(int B) { return B % 12 == 0 && pub_count<SSM>(B) >= 3 && pub_count<SSM>(B) != pub_count<SSM>(B - 12); }
template <bool SSM> constexpr int chk_at(int B) { const int e = (Cfg<SSM>::NM - B) / 12; return (B % 12 == 0 && e > 0 && e < 10 && FD[e]) ? e : 0; }
template <bool SSM> constexpr int fld_of(int B) { return B - FD[chk_at<SSM>(B)]; }
template <bool SSM> constexpr bool fld_at(int m) { for (int e = 1; e < 10; ++e) if (FD[e] && Cfg<SSM>::NM - 12 * e - FD[e] == m) return true; return false; }
template <bool SSM> constexpr int job_at(int m) {
  const int e = (Cfg<SSM>::NM - (m - m % 12)) / 12, k = (m % 12) / 4; return (m % 4 == 2 && k < jcount(e)) ? jfirst(e) + k : -1; }
template <bool SSM> constexpr int xld_at(int m) { const int k = m - (Cfg<SSM>::NM - 12 * 11); return k >= 0 && k < 18 && (k & 1) ? k >> 1 : -1; }
struct Sched { int slot[126]; int istep[126]; int vm[432]; int nis[432]; int fis[432]; int jslot[18]; bool ok; };
template <bool SSM> constexpr Sched make_sched() {
  using C = Cfg<SSM>;
  Sched s{};
  int free_at[19] = {}, b0[19] = {}, b1[19] = {};
  for (int i = 0; i < 19; ++i) { free_at[i] = i < 2 * NXP ? ((i >= 2 * NXP - 8 && i < 10) ? 20 + 4 * (i >> 1) : 4 + 4 * (i >> 1)) : -1; b0[i] = 1000; b1[i] = 1000; }
  if (SSM) { for (int i = HSLOT; i < 18; ++i) { b0[i] = i >= KSLOT ? 72 : C::NM1; b1[i] = C::NM1 + (i < HSLOT + 7 ? 0 : 12); } }
  else { b0[17] = b0[18] = 72; b1[17] = b1[18] = 240; b0[15] = b0[16] = 108; b1[15] = b1[16] = 252; b0[13] = b0[14] = 144; b1[13] = b1[14] = 204; b0[11] = b0[12] = 180; b1[11] = b1[12] = 216; }
  int q = 0; bool jok = true;
  for (int B = -1; B < C::NM; B = B < 0 ? 0 : next_bar(B)) {
    const int len = B < 0 ? I0 : next_bar(B) - B, maxi = B < 0 ? I0 : len == 4 ? 1 + ((B >> 2) & 1) : 4;
    if (B >= 36) for (int k = 0; k < jcount((C::NM - B) / 12); ++k) {
      int best = -1; for (int i = 0; i < C::NSLOT; ++i) if (free_at[i] <= B) { best = i; break; }
      if (best < 0) jok = false; else { s.jslot[JORD[jfirst((C::NM - B) / 12) + k]] = best; free_at[best] = 100000; } }
    int n = 0;
    while (n < maxi && q + n < C::NI && item_piece(q + n) < (B < 0 ? 0 : B) / 4 + 18) {
      const int it = q + n, end = next_bar(4 * item_piece(it) + 3); int best = -1;
      int key = 100000;
      for (int i = C::NSLOT - 1; i >= 0; --i) if (free_at[i] <= B && (end <= b0[i] || B >= b1[i]) && !(SSM && it >= C::NP1 - 14 && it < C::NP1 && (i >= HSLOT) != (it >= C::NP1 - 6))) { const int k2 = B >= b1[i] ? 1000 : b0[i]; if (k2 < key) { key = k2; best = i; } }
      if (best < 0) break;
      s.slot[it] = best; free_at[best] = end; ++n;
    }
    for (int k = 0; k < n; ++k) s.istep[q + k] = B < 0 ? -1 : len == 4 ? B + 2 * k + 1 : B + (n <= 3 ? 4 * k + 3 : 3 * k + 1);
    q += n;
  }
  for (int k = NJIN; k < 18; ++k) { int best = -1; for (int i = 0; i < C::NSLOT; ++i) if (free_at[i] <= C::NM) { best = i; break; }
    if (best < 0) jok = false; else { s.jslot[JORD[k]] = best; free_at[best] = 100000; } }
  s.ok = q == C::NI && jok;
  for (int m = 0; m < C::NM; ++m) { s.nis[m] = 0; s.fis[m] = 0; }
  for (int it = C::NI - 1; it >= 0; --it) if (s.istep[it] >= 0) { s.nis[s.istep[it]]++; s.fis[s.istep[it]] = it; }
  for (int B = 0; B < C::NM; B = next_bar(B)) {
    int np = next_bar(B) / 4; if (np > C::NP - 1) np = C::NP - 1;
    const int need = witem(np);
    if (s.istep[need] >= B) { s.ok = false; continue; }
    int cnt = 0;
    for (int it = need + 1; it < C::NI; ++it) if (s.istep[it] < B) ++cnt;
    for (int m = s.istep[need] + 1; m < B; ++m) if (m >= 0) cnt += (store_of<SSM>(m) >= 0) + (pf_of(m) >= 0) + fld_at<SSM>(m) + (job_at<SSM>(m) >= 0) + (xld_at<SSM>(m) >= 0);
    for (int t = 0; t < 2; ++t) {
      const int m0 = t == 0 ? (pub_at<SSM>(B) ? last_store<SSM>(pub_count<SSM>(B) - 1) : -1) : (chk_at<SSM>(B) ? fld_of<SSM>(B) : -1);
      if (m0 < 0) continue;
      int c2 = (t == 0 ? fld_at<SSM>(m0) : 0) + (job_at<SSM>(m0) >= 0) + s.nis[m0];
      for (int m = m0 + 1; m < B; ++m) c2 += (store_of<SSM>(m) >= 0) + fld_at<SSM>(m) + (job_at<SSM>(m) >= 0) + (xld_at<SSM>(m) >= 0) + s.nis[m];
      if (c2 < cnt) cnt = c2;
    }
    s.vm[B] = cnt; if (cnt > 63) s.ok = false;
  }
  return s;
}
template <bool SSM> struct SC { static constexpr Sched s = make_sched<SSM>(); };
static_assert(SC<false>::s.ok && SC<true>::s.ok, "bad schedule");

#define TS(i)

struct Args {
  const u16 *x, *lre, *lim, *bre, *bim, *cre, *cim, *ldt, *dsk, *w1, *b1, *w2, *b2;
  char* ws; float* out;
};

typedef const __attribute__((address_space(4))) Args* KArgs;
DI KArgs kargs() { KArgs p = (KArgs)__builtin_amdgcn_kernarg_segment_ptr(); asm volatile("" : "+s"(p)); return p; }
template <bool SSM> DI void body(const unsigned lds0, const int slice, const int rb) {
  using C = Cfg<SSM>;
#define S SC<SSM>::s
  Args a;
  { KArgs k = kargs(); a.x = k->x; a.ws = k->ws;
    if constexpr (SSM) { a.lre = k->lre; a.lim = k->lim; a.ldt = k->ldt; a.bre = k->bre; a.bim = k->bim; a.cre = k->cre; a.cim = k->cim; }
    else { a.w1 = k->w1; a.b1 = k->b1; a.w2 = k->w2; }
    a.b2 = k->b2; a.dsk = k->dsk; }
  constexpr int PITCH = C::PITCH, TAB = C::MISC, TAB1 = TAB + NSIG * 16, SEG = TAB1 + NSIG * 16, FST = LDS_BYTES - 512, ETAB = FST - 576, CSX = ETAB - 320;
  static_assert(!SSM || SEG + 4 * NSIG * 8 <= CSX, "");
  int tid = threadIdx.x, lane = tid & 63;
  const int w = __builtin_amdgcn_readfirstlane(tid >> 6), r = w & 3, c = w >> 2;
  TS(0);
  const int f0 = slice * FS, t0 = rb * 128;
  const int sg = slice - NFFN, p0 = 128 * sg, p4 = 512 + 32 * (sg & 1);
  int h = lane >> 5, l31 = lane & 31;
  const int drow = 8 * w + (lane >> 3), dcp = lane & 7;
  const unsigned voffX = drow * 1152 + ((dcp ^ ((drow >> 1) & 7)) << 4);
  const char* xb = (const char*)(a.x + (size_t)t0 * D);
  const unsigned ddst = lds0 + w * (SSM ? 1056 : 1024);
  unsigned voff1, voff2; const char *sb1, *sb2;
  if constexpr (SSM) {
    const int sw = dcp ^ ((drow >> 1) & 1);
    voff1 = (16 * c + 8 * (w & 1) + (lane >> 3)) * 1152 + (sw << 4);
    sb1 = (const char*)(((w >> 1) & 1) ? a.bim : a.bre);
    sb2 = (const char*)(c ? a.cim : a.cre);
  } else {
    const unsigned sw = (dcp ^ (((drow >> 1) & 1) << 2)) << 4;
    voff1 = drow * 9216 + sw; voff2 = drow * 1152 + sw;
    sb1 = (const char*)(a.w1 + f0); sb2 = (const char*)(a.w2 + (size_t)f0 * D);
  }
  auto issue = [&](auto it_) {
    constexpr int p = it_, q = p - C::NP1, so = S.slot[p] * PITCH;
    if constexpr (SSM) {
      if constexpr (p < C::NP1) dma1(voff1, sb1 + (p / 9 < 4 ? p0 + (p / 9) * 32 : p4) * 1152 + (p % 9) * 128, ddst + so);
      else {
        if constexpr (q == 0) { int tz = threadIdx.x; PIN(tz); const int dr = (tz >> 2) & 63, d8 = tz >> 3;
          voff2 = dr * 1152 + (((tz & 3) ^ ((dr >> 3) & 1)) << 4); voff1 = d8 * 1152 + (((tz & 7) ^ ((d8 >> 1) & 1)) << 4); }
        constexpr int dp = q / NCH, j = q % NCH;
        if constexpr (j < 4) dma1(voff1, (const char*)((j & 1) ? a.cim : a.cre) + dp * (64 * 1152) + p0 * 2 + (j >> 1) * 128, ddst + so);
        else dma1(voff2, sb2 + dp * (64 * 1152) + p4 * 2, ddst + so);
      }
    } else {
      if constexpr (p < C::NP1) dma1(voff1, sb1 + (p / 9) * 128 + (size_t)(p % 9) * (64 * 9216), ddst + so);
      else dma1(voff2, sb2 + p2_dp<SSM>(q) * 128 + p2_ch<SSM>(q) * (64 * 1152), ddst + so);
    }
  };
  u32x2 braw; unsigned plr, pli, pdt, tag, rtag;
  if constexpr (SSM) {
    const int sp = tid < 128 ? p0 + tid : p4 + (tid & 31);
    asm volatile("global_load_ushort %0, %1, off" : "=v"(plr) : "v"(a.lre + sp) : "memory");
    asm volatile("global_load_ushort %0, %1, off" : "=v"(pli) : "v"(a.lim + sp) : "memory");
    asm volatile("global_load_ushort %0, %1, off" : "=v"(pdt) : "v"(a.ldt + sp) : "memory");
    asm volatile("global_load_dword %0, %1, off sc1" : "=v"(tag) : "v"(a.ws + WS_EPOCH + sg * 256) : "memory");
  } else {
    asm volatile("global_load_dwordx2 %0, %1, off" : "=v"(braw) : "v"(a.b1 + f0 + 4 * (tid < 96 ? tid : 95)) : "memory");
  }
  asm volatile("global_load_dword %0, %1, off sc1" : "=v"(rtag) : "v"(a.ws + WS_REPOCH + rb * 256) : "memory");
  const int jd0 = (slice >> 3) * 32 + (slice & 1) * 16, jt0 = ((slice >> 1) & 3) * 32;
  unsigned eb2, edk;
  { const int ti = min(tid, 143), d = (ti >> 4) * 64 + jd0 + (ti & 15);
    asm volatile("global_load_ushort %0, %1, off" : "=v"(eb2) : "v"(a.b2 + d) : "memory");
    asm volatile("global_load_ushort %0, %1, off" : "=v"(edk) : "v"(a.dsk + d) : "memory"); }
  { const char* pc; asm volatile("s_getpc_b64 %0" : "=s"(pc));
    const unsigned vo = min(lane, 4) * 128;
    asm volatile("s_mov_b32 m0, %2\n\ts_nop 0\n\tglobal_load_lds_dword %0, %1" :: "v"(vo), "s"(pc + ((rb & 7) * 8 + w) * (CODEPF / 64)), "s"(lds0 + LDS_BYTES - 256) : "memory"); }
  u32x4 xr[18];
  sfor<0, 9>([&](auto k_) { gload16<k_ * 128>(xr[k_], voffX, xb); });
  u32x4 xf[36];
  unsigned xa[2][4];
  {
    const int xrow = 32 * (r & 1) + (SSM ? 16 * ((l31 >> 2) & 1) + (l31 & 3) + 4 * (l31 >> 3) : l31), xkey = (xrow >> 1) & 7;
#pragma unroll
    for (int kk = 0; kk < 4; ++kk) { xa[0][kk] = lds0 + (r >> 1) * PITCH + xrow * 128 + (((2 * kk + h) ^ xkey) << 4); xa[1][kk] = xa[0][kk] + 8 * PITCH; PIN(xa[0][kk]); PIN(xa[1][kk]); } }
  unsigned fb1[3], fb2[3];
  if constexpr (SSM) {
    const int row = 32 * c + l31;
#pragma unroll
    for (int i = 0; i < 3; ++i) fb1[i] = lds0 + (row >> 3) * 1056 + (row & 7) * 128 + ((h ^ ((row >> 1) & 1)) << 4) + i * 65536;
  } else {
    const int q4 = (lane & 15) >> 2, pq = lane & 3, cb = (lane >> 4) & 1;
    const unsigned colb = (((c * 4 + cb * 2 + (pq >> 1)) ^ ((q4 >> 1) << 2)) << 4) + 8 * (pq & 1);
#pragma unroll
    for (int i = 0; i < 3; ++i) { fb1[i] = lds0 + (8 * h + q4) * 128 + colb + i * 65536; fb2[i] = lds0 + (4 * h + q4) * 128 + colb + i * 65536; }
  }
  PIN(fb1[0]); PIN(fb1[1]); PIN(fb1[2]); PIN(voff1);
  auto fread = [&](auto m_) -> bf16x8 {
    constexpr int p = m_ / 4, kk = m_ & 3;
    constexpr bool mixed = SSM && p >= C::NP1 && (p - C::NP1) % NCH == 4;
    constexpr int off = S.slot[p] * PITCH + (!SSM ? kk * 2048 : !mixed ? kk * 32 : (kk >> 1) * 4224 + (kk & 1) * 32);
    if constexpr (SSM && m_ == C::NM1 + 16) { int tz = threadIdx.x; PIN(tz); const int row = 32 * c + (tz & 31);
#pragma unroll
      for (int i = 0; i < 3; ++i) { fb2[i] = lds0 + (row >> 4) * 1056 + (row & 15) * 64 + ((((tz >> 5) ^ (row >> 3)) & 1) << 4) + i * 65536; pin1(fb2[i]); } }
    if constexpr (mixed) return __builtin_bit_cast(bf16x8, lds128(fb2[off >> 16] + (off & 65535)));
    else if constexpr (SSM) return __builtin_bit_cast(bf16x8, lds128(fb1[off >> 16] + (off & 65535)));
    else if constexpr (p < C::NP1) return cat(trd(fb1[off >> 16] + (off & 65535)), trd(fb1[off >> 16] + (off & 65535) + 512));
    else return cat(trd(fb2[off >> 16] + (off & 65535)), trd(fb2[off >> 16] + (off & 65535) + 1024)); };
  auto xread = [&](int m) { const int sl = xslot(m >> 2); xf[m] = lds128(xa[sl >> 3][m & 3] + (sl & 7) * PITCH); };
  unsigned pwr, prd, biasb;
  if constexpr (!SSM) { pwr = lds0 + w * 2048 + lane * 16 + 11 * PITCH; prd = lds0 + r * 2048 + lane * 16 + 11 * PITCH; PIN(pwr);
    biasb = lds0 + TAB + (32 * c + 4 * h) * 4; PIN(biasb); }
  asm volatile("s_waitcnt vmcnt(6)" ::: "memory");
  sfor<0, 9>([&](auto k_) { gload16<k_ * 128>(xr[9 + k_], voffX, xb + 64 * 1152); });
  PIN(rtag); PIN(eb2); PIN(edk);
  rtag = __builtin_amdgcn_readfirstlane(rtag) + 1u;
  *(LAS unsigned*)(lds0 + ETAB + min(tid, 143) * 4) = eb2 | (edk << 16);
  if constexpr (SSM) {
    PIN(plr); PIN(pli); PIN(pdt); PIN(tag);
    if (tid < NSIG) {
      const float lr = lo2f(plr), li = lo2f(pli), dt = rbf(expf(lo2f(pdt)));
      const float e = expf(dt * lr); float sn, cs; sincosf(dt * li, &sn, &cs);
      const float ar = e * cs, ai = e * sn, nr = ar - 1.f, den = lr * lr + li * li;
      const float k2 = (sg >= 2 && tid >= 128) ? 0.f : 2.f / den;
      *(LAS f32x4*)(lds0 + TAB + tid * 16) = (f32x4){ar, ai, (nr * lr + ai * li) * k2, (ai * lr - nr * li) * k2};
      const cf a8 = csq(csq(csq(cf{ar, ai}))), a16 = csq(a8);
      *(LAS f32x4*)(lds0 + TAB1 + tid * 16) = (f32x4){a8.r, a8.i, a16.r, a16.i};
    }
    tag = __builtin_amdgcn_readfirstlane(tag) + 1u;
  } else {
    PIN(braw);
    if (tid < 96) *(LAS f32x4*)(lds0 + TAB + tid * 16) = (f32x4){BETA * lo2f(braw[0]), BETA * hi2f(braw[0]), BETA * lo2f(braw[1]), BETA * hi2f(braw[1])};
  }
  unsigned xw[2] = {lds0 + tid * 16, lds0 + tid * 16 + 7 * PITCH}; PIN(xw[0]); PIN(xw[1]);
  auto xput = [&](int k) { const int sl = xslot(k % 9) + k / 9; *(LAS u32x4*)(xw[sl / 7] + (sl % 7) * PITCH) = xr[k]; };
  u32x2 ones = {0x3f803f80u, 0x3f803f80u}; PIN(ones);
  sfor<0, 2>([&](auto hf_) { constexpr int hf = hf_;
    f32x4 sa[2] = {}, sq[2] = {};
    sfor<0, 3>([&](auto g_) { constexpr int k0 = 9 * hf + 3 * g_;
      wait3<15 - k0 + hf * I0>(xr + k0);
#pragma unroll
      for (int k = k0; k < k0 + 3; ++k)
#pragma unroll
        for (int e = 0; e < 2; ++e) { const u32x2 v = {xr[k][2 * e], xr[k][2 * e + 1]}; sa[e] = mf4(ones, v, sa[e]); sq[e] = mf4(v, v, sq[e]); } });
    const f32x4 q4 = sq[0] + sq[1];
    float d0 = q4[0], d1 = q4[1], d2 = q4[2], d3 = q4[3]; PIN(d1); PIN(d3);
    d0 = (lane & 1) ? d1 : d0; d2 = (lane & 1) ? d3 : d2;
    float s1 = sa[0][0] + sa[1][0], s2 = (lane & 2) ? d2 : d0;
    s1 += dpp<0xB1>(s1); s2 += dpp<0xB1>(s2); s1 += dpp<0x4E>(s1); s2 += dpp<0x4E>(s2); s1 += dpp<0x141>(s1); s2 += dpp<0x141>(s2);
    const float mean = s1 * (1.f / D);
    const float var = fmaxf(s2 * (1.f / D) - mean * mean, 0.f);
    const float mb = rbf(mean), iden = __builtin_amdgcn_rcpf(rbf(__builtin_amdgcn_sqrtf(rbf(rbf(var) + 1e-5f))));
    const float inv = (SSM ? 1.f : BETA) * iden, ih = rbf(inv), il = rbf(inv - ih), nb = -mb * (ih + il);
    if (((64 * hf + 8 * w) >> 5) == (jt0 >> 5)) *(LAS f32x2*)(lds0 + FST + (drow & 31) * 8) = (f32x2){mb, iden};
    const unsigned bh = __float_as_uint(ih) >> ((lane & 1) ? 0 : 16), bl = __float_as_uint(il) >> ((lane & 1) ? 0 : 16);
    const u32x2 Ah = {(lane & 2) ? 0u : bh, (lane & 2) ? bh : 0u}, Al = {(lane & 2) ? 0u : bl, (lane & 2) ? bl : 0u};
    const f32x4 nv = {nb, nb, nb, nb};
#pragma unroll
    for (int k3 = 9 * hf; k3 < 9 * hf + 9; k3 += 3) {
      f32x4 t[6];
#pragma unroll
      for (int e = 0; e < 6; ++e) t[e] = mf4(Al, (u32x2){xr[k3 + (e >> 1)][2 * (e & 1)], xr[k3 + (e >> 1)][2 * (e & 1) + 1]}, nv);
#pragma unroll
      for (int e = 0; e < 6; ++e) t[e] = mf4(Ah, (u32x2){xr[k3 + (e >> 1)][2 * (e & 1)], xr[k3 + (e >> 1)][2 * (e & 1) + 1]}, t[e]);
#pragma unroll
      for (int e = 0; e < 6; ++e) { xr[k3 + (e >> 1)][2 * (e & 1)] = pk2(t[e][0], t[e][1]); xr[k3 + (e >> 1)][2 * (e & 1) + 1] = pk2(t[e][2], t[e][3]); }
#pragma unroll
      for (int k = k3; k < k3 + 3; ++k) if (k % 9 < NXP) xput(k);
    }
    if constexpr (hf == 0) sfor<0, I0>(issue);
  });
  wait_bar<S.vm[0], 0>();
  TS(3);
  bf16x8 af[4];
  f32x16 acc, prev, oacc[9];
  u32x4 P[6][4], pn[2], nbias[4];
  af[0] = fread(ic<0>{}); xread(0);
  if constexpr (!SSM) {
#pragma unroll
    for (int g = 0; g < 4; ++g) nbias[g] = lds128(biasb + g * 32); }
  af[1] = fread(ic<1>{}); xread(1); af[2] = fread(ic<2>{}); xread(2);
  cf u[NCH][8];
  const f32x16 zero16 = {};
  float gt0, gt1;
  char* pbase; unsigned pvo, eo; float xv[9];
  auto gelu_ph = [&](int e, int ph) {
    const float v0 = prev[2 * e], v1 = prev[2 * e + 1];
    if (ph == 0) { gt0 = v0 * __builtin_fmaf(v0, v0, KAP); gt1 = v1 * __builtin_fmaf(v1, v1, KAP); PIN(gt0); PIN(gt1); }
    if (ph == 1) { gt0 = __builtin_amdgcn_exp2f(-gt0); gt1 = __builtin_amdgcn_exp2f(-gt1); PIN(gt0); PIN(gt1); }
    if (ph == 2) { gt0 = __builtin_amdgcn_rcpf(1.f + gt0); gt1 = 1.f + gt1; PIN(gt0); PIN(gt1); }
    if (ph == 3) { unsigned pv = pk2(v0 * gt0, v1 * __builtin_amdgcn_rcpf(gt1)); PIN(pv); pn[e >> 2][e & 3] = pv; }
  };
  cf sE, sH, sU;
  f32x4 stab, stab1;
  unsigned segw;
#define PINC(z) do { if (pin) { PIN((z).r); PIN((z).i); } } while (0)
  auto pass1 = [&](int ch, int k, bool pin) {
    if (k == 0) { int tz = threadIdx.x; PIN(tz); segw = lds0 + (32 * ch + 16 * c + (tz & 15)) * 8;
      stab = *(LAS f32x4*)(2 * segw - lds0 + TAB); }
    const cf av = {stab[0], stab[1]};
    if (k >= 1 && k <= 4) { for (int j = 2 * k - 2; j < 2 * k; ++j) { swap16(prev[j], prev[j + 8], u[ch][j].r, u[ch][j].i); PINC(u[ch][j]); } }
    if (k >= 5 && k <= 11) { const int i = k - 5; sE = cmad(av, i ? sE : u[ch][0], u[ch][i + 1]); PINC(sE); }
    if (k == 9) stab1 = *(LAS f32x4*)(2 * segw - lds0 + TAB1);
    const cf a8 = {stab1[0], stab1[1]}, a16 = {stab1[2], stab1[3]};
    if (k == 12) { swap16(sE.r, sE.r, sE.r, sH.r); swap16(sE.i, sE.i, sE.i, sH.i); PINC(sH); PINC(sE); }
    if (k == 13) { sU = cmad(a8, sE, sH); PINC(sU); }
    if (k == 14) { swap32(sU.r, sU.r, sH.r); swap32(sU.i, sU.i, sH.i); PINC(sH); PINC(sU); }
    if (k == 15) { cf Sg = cmad(a16, sU, sH); PINC(Sg); *(LAS f32x2*)(segw + SEG + r * NSIG * 8) = (f32x2){Sg.r, Sg.i}; }
    if (k == 16) { sU = {selm(0.f, sU.r, HMASK), selm(0.f, sU.i, HMASK)}; sE = cmad(a8, sU, sE); PINC(sU); PINC(sE); }
    if (k == 17) { u[ch][0] = cmad(av, cf{selm(sU.r, sE.r, GMASK), selm(sU.i, sE.i, GMASK)}, u[ch][0]); PINC(u[ch][0]); }
    if (k >= 18) { const int j = k - 18; u[ch][j] = cmul(cf{stab[2], stab[3]}, u[ch][j]); PINC(u[ch][j]); }
  };
  char* const granb = a.ws + WS_GRAN + (size_t)(sg * 16 + 15 - rb) * (NSIG * 16);
  auto seg_rd = [&](int t, cf* sv) {
#pragma unroll
    for (int i = 0; i < 4; ++i) { const f32x2 v = *(LAS f32x2*)(lds0 + SEG + (i * NSIG + t) * 8); sv[i] = {v[0], v[1]}; } };
  auto pow32 = [&](int t) { const f32x2 v = *(LAS f32x2*)(lds0 + TAB1 + t * 16 + 8); return csq(cf{v[0], v[1]}); };
  auto carry_out = [&](int t, cf a32) { cf sv[4]; seg_rd(t, sv);
    const cf T = cmad(a32, cmad(a32, cmad(a32, sv[0], sv[1]), sv[2]), sv[3]);
    __builtin_amdgcn_raw_buffer_store_b128((u32x4){__float_as_uint(T.r), tag, __float_as_uint(T.i), tag}, __builtin_amdgcn_make_buffer_rsrc(granb, 0, NSIG * 16, 0x00020000), t * 16, 0, 16); };
  auto carry_ld = [&](int t, u32x4* v) {
    const auto rs = __builtin_amdgcn_make_buffer_rsrc(granb + NSIG * 16, 0, rb * (NSIG * 16), 0x00020000);
    unsigned vo = t * 16; PIN(vo);
#pragma unroll
    for (int k = 0; k < 15; ++k) v[k] = __builtin_amdgcn_raw_buffer_load_b128(rs, vo + k * (NSIG * 16), 0, 16); };
  auto carry_in = [&](int t, cf a32, u32x4* v) {
    cf sv[4]; seg_rd(t, sv);
    const cf a128 = csq(csq(a32)), a512 = csq(csq(a128));
    for (unsigned spins = 0; spins < (1u << 16); ++spins) {
      unsigned sum = 0;
#pragma unroll
      for (int k = 0; k < 15; ++k) sum += v[k][1] + v[k][3];
      if (__all(sum == 2u * rb * tag)) break;
      carry_ld(t, v);
    }
    cf q[4];
#pragma unroll
    for (int i = 3; i >= 0; --i)
#pragma unroll
      for (int n = 0; n < 4; ++n) { const int k = 4 * n + i; if (k > 14) continue;
        const cf x = {__uint_as_float(v[k][0]), __uint_as_float(v[k][2])}; q[n] = (i == 3 || k == 14) ? x : cmad(a128, q[n], x); }
    cf G = cmad(a512, cmad(a512, cmad(a512, q[3], q[2]), q[1]), q[0]);
#pragma unroll
    for (int i = 0; i < 4; ++i) { *(LAS f32x2*)(lds0 + SEG + (i * NSIG + t) * 8) = (f32x2){G.r, G.i}; G = cmad(a32, G, sv[i]); } };
  unsigned hr[2][2];
  auto hread = [&](int n) { const int ch = h_ch(n), kk = h_kk(n); P[ch][kk] = __builtin_bit_cast(u32x4, cat(trd(hr[ch >> 2][0] + (ch & 3) * 16384 + kk * 1024), trd(hr[ch >> 2][1] + (ch & 3) * 16384 + kk * 1024))); };
  auto ssm_boundary = [&]() {
    tid = threadIdx.x; PIN(tid); lane = tid & 63; h = lane >> 5;
    const int ts = min(tid, NSIG - 1);
    u32x4 v[15];
    if (w < 2) carry_ld(ts, v);
    const cf a32 = pow32(ts);
#pragma unroll
    for (int k = 0; k < 16; ++k) pass1(NCH - 1, k, false);
    const int g = (lane >> 4) & 1, s16 = lane & 15;
#pragma unroll
    for (int k = 0; k < 4; ++k) { const f32x4 t = *(LAS f32x4*)(lds0 + KSLOT * PITCH + tid * 16 + k * 8192); u[0][2 * k] = {t[0], t[1]}; u[0][2 * k + 1] = {t[2], t[3]}; }
    SBAR();
    if (w < 2) carry_in(ts, a32, v);
    wait_bar<63, 0>();
    TS(8);
    if (w == 2) carry_out(ts, a32);
    const int q4 = (lane & 15) >> 2, pq = lane & 3;
    unsigned hw[2];
    hw[0] = lds0 + HSLOT * PITCH + r * 4096 + (16 * c + s16) * 64 + (((2 * h + g) ^ ((s16 >> 1) & 3)) << 4); hw[1] = hw[0] + 65536;
    PIN(hw[0]); PIN(hw[1]);
#pragma unroll
    for (int k = 0; k < 2; ++k) {
      hr[0][k] = lds0 + HSLOT * PITCH + r * 4096 + (8 * h + 4 * k + q4) * 64 + (((2 * g + (pq >> 1)) ^ (2 * k + (q4 >> 1))) << 4) + (pq & 1) * 8; hr[1][k] = hr[0][k] + 65536;
      PIN(hr[0][k]); PIN(hr[1][k]); }
    cf av[NCH], pw[NCH];
    auto prep = [&](int ch) {
      const f32x4 t = *(LAS f32x4*)(lds0 + TAB + (32 * ch + 16 * c + s16) * 16), t1 = *(LAS f32x4*)(lds0 + TAB1 + (32 * ch + 16 * c + s16) * 16);
      av[ch] = {t[0], t[1]};
      pw[ch] = cmul(cmul(cf{t[2], t[3]}, cf{selm(1.f, t1[0], GMASK), selm(0.f, t1[1], GMASK)}), cf{selm(1.f, t1[2], HMASK), selm(0.f, t1[3], HMASK)}); };
    auto fin = [&](int c0, int c1) {
      f32x2 z[NCH], o[NCH][8];
#pragma unroll
      for (int ch = c0; ch < c1; ++ch) { const f32x2 ci = *(LAS f32x2*)(lds0 + SEG + (r * NSIG + 32 * ch + 16 * c + s16) * 8);
        const cf z0 = cmul(pw[ch], cf{ci[0], ci[1]}); z[ch] = (f32x2){z0.r, z0.i}; }
#pragma unroll
      for (int i = 0; i < 8; ++i)
#pragma unroll
        for (int ch = c0; ch < c1; ++ch) {
          const f32x2 t = __builtin_elementwise_fma((f32x2){av[ch].r, av[ch].r}, z[ch], (f32x2){u[ch][i].r, u[ch][i].i});
          z[ch] = __builtin_elementwise_fma((f32x2){-av[ch].i, av[ch].i}, __builtin_shufflevector(z[ch], z[ch], 1, 0), t); o[ch][i] = z[ch]; }
#pragma unroll
      for (int ch = c0; ch < c1; ++ch) {
        *(LAS u32x4*)(hw[ch >> 2] + (ch & 3) * 16384) = (u32x4){pk2(o[ch][0][0], o[ch][1][0]), pk2(o[ch][2][0], o[ch][3][0]), pk2(o[ch][4][0], o[ch][5][0]), pk2(o[ch][6][0], o[ch][7][0])};
        *(LAS u32x4*)(hw[ch >> 2] + (ch & 3) * 16384 + 2048) = (u32x4){pk2(-o[ch][0][1], -o[ch][1][1]), pk2(-o[ch][2][1], -o[ch][3][1]), pk2(-o[ch][4][1], -o[ch][5][1]), pk2(-o[ch][6][1], -o[ch][7][1])};
      } };
#pragma unroll
    for (int ch = 0; ch < NCH; ++ch) prep(ch);
    fin(0, 4);
    SBAR();
    if (w == 2) carry_ld(ts, v);
#pragma unroll
    for (int k = 16; k < 26; ++k) pass1(NCH - 1, k, false);
    SBAR();
    TS(9);
    if (w == 2) carry_in(ts, a32, v);
    wait_bar<63, 0>();
    if (rb == 15 && tid == 0) __hip_atomic_store((gu32*)(a.ws + WS_EPOCH + sg * 256), tag, RLX_AGENT);
#pragma unroll
    for (int n = 0; n < 16; ++n) hread(n);
    fin(4, 5);
  };
  unsigned pfo;
  { const int ln = min(lane, 5); pfo = SSM ? (ln & 1) * 1152 + (ln < 4 ? p0 * 2 + (ln & 2) * 64 : 1024) : ln * 128; PIN(pfo); }
  char* const flagb = a.ws + WS_FLAG + rb * 4096;
  u32x2 fl;
  auto publish = [&](unsigned n) { flag_store(flagb + slice * 256, n, rtag, w); };
  auto await = [&](u32x2 v, unsigned n0, unsigned n1) {
    const unsigned need = (pvo & 512) ? n1 : n0;
    if (__builtin_expect(!__all(need == 0 || (v[1] == rtag && v[0] >= need)), 0)) {
      for (unsigned spins = 0; spins < (1u << 16); ++spins) {
        const unsigned long long t = __hip_atomic_load((gu64*)(flagb + w * 256 + (pvo & 512) * 4), RLX_AGENT);
        if (__all(need == 0 || ((unsigned)(t >> 32) == rtag && (unsigned)t >= need))) break;
        __builtin_amdgcn_s_sleep(4);
      }
    } };
  const char* const jsrc = a.ws + WS_PART + (size_t)(w * 16 + rb) * PART_BLK + slice * 1024;
  auto jissue = [&](auto j_) { constexpr int j = j_, so = S.jslot[j] * PITCH;
    dma1s(pvo, jsrc + (j >> 1) * 16384 + (size_t)(j & 1) * (128 * PART_BLK), lds0 + w * 1024 + so); };
  sfor<0, C::NM / 4>([&](auto p_) { sfor<4 * p_, 4 * p_ + 4>([&](auto m_) {
    constexpr int m = m_;
    constexpr bool ph1 = m < C::NM1;
    constexpr int un = m / 36, gn = m % 36, m2 = m - C::NM1;
    if constexpr (m == 36) { TS(4); }
    if constexpr (m == C::NM1) { TS(5);
      if constexpr (SSM) { ssm_boundary(); TS(12); }
      int tz = threadIdx.x; PIN(tz);
      pvo = (tz & 63) * 16; eo = (jt0 + (tz >> 4)) * D + jd0 + (tz & 15); pbase = a.ws + WS_PART + (size_t)(slice * 16 + rb) * PART_BLK + w * 2048; }
    if constexpr (!SSM && m == 180) { PIN(fb2[0]); PIN(fb2[1]); PIN(fb2[2]); PIN(prd); }
    if constexpr (m > 0 && bar_at(m)) {
      constexpr bool lgk0 = (SSM ? m == C::NM1 || m == C::NM1 - 24 : m == 204 || m == 240);
      wait_bar<S.vm[m], (lgk0 ? 0 : 15)>();
      if constexpr (pub_at<SSM>(m)) { constexpr int pc = pub_count<SSM>(m); publish(pc); }
      if constexpr (chk_at<SSM>(m)) { constexpr int n0 = N0[chk_at<SSM>(m)], n1 = N1[chk_at<SSM>(m)]; pin2(fl); await(fl, n0, n1); }
    }
    constexpr bool post = m >= 36 && (ph1 || (!SSM && m2 < 36));
    if constexpr (m + 3 < C::NM) af[(m + 3) & 3] = fread(ic<m + 3>{});
    if constexpr (m + 3 < 36) xread(m + 3);
    if constexpr (SSM && m2 >= 0 && m2 < 4) hread(m2 + 16);
    if constexpr (!SSM) {
      if constexpr (m >= 192 && m < 204) { constexpr int n = m - 192; P[n >> 2][n & 3] = lds128(prd + (pslot(n >> 2) - 11) * PITCH + ((n & 3) >> 1) * PITCH + (n & 1) * 1024); }
      if constexpr (m >= 204 && m < 208) { constexpr int n = m - 204; P[3][n] = lds128(prd + (pslot(3) - 11) * PITCH + (n >> 1) * PITCH + (n & 1) * 1024); }
      if constexpr (m >= 228 && m < 232) { constexpr int n = m - 228; P[4][n] = lds128(prd + (pslot(4) - 11) * PITCH + (n >> 1) * PITCH + (n & 1) * 1024); }
      if constexpr (m >= 240 && m < 244) { constexpr int n = m - 240; P[5][n] = lds128(prd + (pslot(5) - 11) * PITCH + (n >> 1) * PITCH + (n & 1) * 1024); }
      if constexpr (ph1 && un < 5 && gn >= 30 && gn < 34) nbias[gn - 30] = lds128(biasb + (un + 1) * 256 + (gn - 30) * 32);
    }
    if constexpr (ph1) {
      if constexpr (SSM) acc = MFMA(__builtin_bit_cast(bf16x8, xf[gn]), af[m & 3], gn == 0 ? zero16 : acc);
      else {
        if constexpr (gn == 0) {
#pragma unroll
          for (int e = 0; e < 16; ++e) acc[e] = __uint_as_float(nbias[e >> 2][e & 3]); }
        acc = MFMA(af[m & 3], __builtin_bit_cast(bf16x8, xf[gn]), acc); }
    } else {
      constexpr int dp = p2_dp<SSM>(m2 / 4), ch = p2_ch<SSM>(m2 / 4);
      constexpr int bc = SSM ? h_ch(m2 % C::UNIT2) : ch, bk = SSM ? h_kk(m2 % C::UNIT2) : m & 3;
      oacc[dp] = MFMA(af[m & 3], __builtin_bit_cast(bf16x8, P[bc][bk]), (ch == 0 && (m & 3) == 0) ? zero16 : oacc[dp]);
    }
    SBAR();
    if constexpr (m >= 4 * NXP - 12 && m < 24 && (m & 1) == 0) xput(3 + (m >> 2) + 9 * ((m >> 1) & 1));
    if constexpr (ph1 && gn == 35) prev = acc;
    if constexpr (SSM) {
      if constexpr (m == 72) { int tz = threadIdx.x; PIN(tz);
#pragma unroll
        for (int k = 0; k < 4; ++k) *(LAS f32x4*)(lds0 + KSLOT * PITCH + tz * 16 + k * 8192) = (f32x4){u[0][2 * k].r, u[0][2 * k].i, u[0][2 * k + 1].r, u[0][2 * k + 1].i}; }
      if constexpr (post && gn < 35) {
        constexpr int k0 = un < NCH - 1 ? gn * 26 / 35 : gn < 12 ? gn * 16 / 12 : 16 + (gn - 12) * 10 / 23, k1 = un < NCH - 1 ? (gn + 1) * 26 / 35 : gn < 11 ? (gn + 1) * 16 / 12 : 16 + (gn - 11) * 10 / 23;
        sfor<k0, k1>([&](auto k_) { pass1(un - 1, k_, true); }); }
      if constexpr (m == C::NM1 - 24) { int tz = threadIdx.x; PIN(tz); if (w < 2) carry_out(tz, pow32(tz)); } }
    else {
      if constexpr (m >= 72 && m <= 217 && gn < 2) *(LAS u32x4*)(pwr + (pslot(un - 2) - 11) * PITCH + gn * 1024) = pn[gn];
      if constexpr (post && ph1 && gn >= 2 && gn < 34) gelu_ph((gn - 2) >> 2, (gn - 2) & 3);
      if constexpr (post && !ph1 && gn >= 2 && gn < 20) { constexpr int k0 = (gn - 2) * 16 / 9, k1 = (gn - 1) * 16 / 9; sfor<k0, k1>([&](auto k_) { gelu_ph(k_ >> 2, k_ & 3); }); }
      if constexpr (m2 == 20 || m2 == 21) *(LAS u32x4*)(pwr + (pslot(5) - 11) * PITCH + (m2 - 20) * 1024) = pn[m2 - 20];
    }
    if constexpr (store_of<SSM>(m) >= 0) {
      constexpr int dp = store_of<SSM>(m) >> 1, e = (store_of<SSM>(m) & 1) * 8;
      u32x4 o = {pk2(oacc[dp][e], oacc[dp][e + 1]), pk2(oacc[dp][e + 2], oacc[dp][e + 3]), pk2(oacc[dp][e + 4], oacc[dp][e + 5]), pk2(oacc[dp][e + 6], oacc[dp][e + 7])};
      pst(pvo, pbase + dp * 16384 + (e >> 3) * 1024, o);
    }
    if constexpr (pf_of(m) >= 0) {
      const char* sb;
      if constexpr (SSM) { sb = sb2 + ((rb & 7) * 72 + r * 18 + pf_of(m) * 2) * 1152; }
      else sb = sb2 + ((rb & 7) * 432 + w * 54 + pf_of(m) * 6) * 128;
      touch(pfo, sb, lds0 + LDS_BYTES - 256);
    }
    if constexpr (xld_at<SSM>(m) >= 0) { constexpr int dp = xld_at<SSM>(m); xv[dp] = 0.f; gload_hi<dp * 128>(xv[dp], eo * 2, xb); }
    if constexpr (fld_at<SSM>(m)) gload8s(fl, (pvo & 512) * 4, flagb + w * 256);
    if constexpr (job_at<SSM>(m) >= 0) jissue(ic<JORD[job_at<SSM>(m) < 0 ? 0 : job_at<SSM>(m)]>{});
    sfor<S.fis[m], S.fis[m] + S.nis[m]>(issue);
    SBAR();
  }); });
  TS(13); TS(6);
#pragma unroll
  for (int hf = 0; hf < 2; ++hf) { const int e = hf * 8;
    u32x4 o = {pk2(oacc[8][e], oacc[8][e + 1]), pk2(oacc[8][e + 2], oacc[8][e + 3]), pk2(oacc[8][e + 4], oacc[8][e + 5]), pk2(oacc[8][e + 6], oacc[8][e + 7])};
    pst(pvo, pbase + 8 * 16384 + hf * 1024, o); }
  gload8s(fl, (pvo & 512) * 4, flagb + w * 256);
  wait_bar<3, 15>();
  publish(8);
  float* const outb = kargs()->out + (size_t)t0 * D;
  tid = threadIdx.x; PIN(tid); lane = tid & 63; h = lane >> 5;
  wait_bar<0, 0>();
  publish(9);
  TS(7);
  pin2(fl); await(fl, MAXPUB, MAXPUB);
  sfor<NJIN, 2 * MAXPUB>([&](auto k_) { jissue(ic<JORD[k_]>{}); });
  const f32x2 stt = *(LAS f32x2*)(lds0 + FST + (tid >> 4) * 8);
  float eb[9], ed[9];
#pragma unroll
  for (int dp = 0; dp < 9; ++dp) { PIN(xv[dp]); const unsigned e = *(LAS unsigned*)(lds0 + ETAB + (dp * 16 + (lane & 15)) * 4);
    eb[dp] = lo2f(e); ed[dp] = rbf(rbf(rbf(xv[dp] - stt[0]) * stt[1]) * hi2f(e)); }
  u32x4 sel[2];
#pragma unroll
  for (int b = 0; b < 2; ++b) { const int row = (lane & 31) - 4 * b; const unsigned o = 0x3f803f80u;
    const unsigned lo = row == 0 ? o : 0u, hi = (row == 0 && !h) || (row == 1 && h) ? o : 0u; sel[b] = (u32x4){lo, lo, hi, hi}; }
  const unsigned jb = lds0 + ((lane & 15) >> 2) * 1024 + (4 * w + ((lane >> 4) & 1) + 32 * (lane & 1)) * 16 + ((lane >> 1) & 1) * 8;
  auto finish = [&](auto d0_, auto d1_) { constexpr int d0 = d0_, d1 = d1_;
    bf16x8 b0[9], b1[9]; f32x16 t[9]; float uu[9], ee[9];
    sfor<d0, d1>([&](auto dp_) { constexpr int dp = dp_;
      const unsigned ad = jb + (h ? S.jslot[2 * dp + 1] : S.jslot[2 * dp]) * PITCH;
      b0[dp] = cat(trd(ad), trd(ad + 4096)); b1[dp] = cat(trd(ad + 32), trd(ad + 4096 + 32)); });
    SBAR();
#pragma unroll
    for (int dp = d0; dp < d1; ++dp) t[dp] = MFMA(__builtin_bit_cast(bf16x8, sel[0]), b0[dp], zero16);
#pragma unroll
    for (int dp = d0; dp < d1; ++dp) t[dp] = MFMA(__builtin_bit_cast(bf16x8, sel[1]), b1[dp], t[dp]);
    SBAR();
#pragma unroll
    for (int dp = d0; dp < d1; ++dp) uu[dp] = rbf(rbf(t[dp][0] * IBETA) + eb[dp]);
#pragma unroll
    for (int dp = d0; dp < d1; ++dp) ee[dp] = __builtin_amdgcn_exp2f(uu[dp] * -1.4426950409f);
#pragma unroll
    for (int dp = d0; dp < d1; ++dp) ee[dp] = __builtin_amdgcn_rcpf(1.f + ee[dp]);
#pragma unroll
    for (int dp = d0; dp < d1; ++dp) outb[eo + dp * 64] = xv[dp] + (t[dp][1] + ed[dp]) * rbf(uu[dp] * ee[dp]);
    SBAR(); };
  constexpr int F0 = NJIN >= 8 ? NJIN / 2 : NJIN >= 7 ? 3 : 0;
  finish(ic<0>{}, ic<F0>{});
  await((u32x2){0u, 0u}, 9, 9);
  TS(10);
  sfor<2 * MAXPUB, 18>([&](auto k_) { jissue(ic<JORD[k_]>{}); });
  wait_bar<18 - 2 * MAXPUB, 15>();
  if (slice == 0 && tid == 0) __hip_atomic_store((gu32*)(a.ws + WS_REPOCH + rb * 256), rtag, RLX_AGENT);
  finish(ic<F0>{}, ic<MAXPUB>{});
  wait_bar<MAXPUB - F0, 15>();
  finish(ic<MAXPUB>{}, ic<9>{});
  TS(11);
}
#undef S
__global__ __launch_bounds__(512, 2) void k_main(Args a) {
  __shared__ __attribute__((aligned(16))) char smem[LDS_BYTES];
  const unsigned lds0 = (unsigned)(uintptr_t)smem;
  const int bid = blockIdx.x, xcd = bid & 7, loc = bid >> 3;
  const int slice = (loc & 3) * 4 + (xcd & 3), rb = (xcd >> 2) * 8 + (loc >> 2);
  if (slice < NFFN) body<false>(lds0, slice, rb); else body<true>(lds0, slice, rb);
}

extern "C" void kernel_launch(void* const* d_in, const int* in_sizes, int n_in, void* d_out, int out_size, void* d_ws, size_t ws_size, hipStream_t stream) {
  Args a;
  a.x = (const u16*)d_in[0]; a.lre = (const u16*)d_in[1]; a.lim = (const u16*)d_in[2]; a.bre = (const u16*)d_in[3]; a.bim = (const u16*)d_in[4];
  a.cre = (const u16*)d_in[5]; a.cim = (const u16*)d_in[6]; a.ldt = (const u16*)d_in[7]; a.dsk = (const u16*)d_in[8];
  a.w1 = (const u16*)d_in[10]; a.b1 = (const u16*)d_in[11]; a.w2 = (const u16*)d_in[12]; a.b2 = (const u16*)d_in[13];
  a.ws = (char*)d_ws; a.out = (float*)d_out;
  k_main<<<256, 512, 0, stream>>>(a);
}
#define NSTAMP 14
#define STAMP_VALID(b) 1
#define STAMP_CLASS(b) ((((b) >> 3) & 3) == 3)
```

```cpp
#define SC1ST
#define CODEPF 28672
#define OSC1
#define NSH 8
#define TG 1
#define ROT 4
#define PF1S 0
#include <hip/hip_runtime.h>
#include <stdint.h>
#include <type_traits>

typedef unsigned short u16;
typedef short bf16x8 __attribute__((ext_vector_type(8)));
typedef short s16x4 __attribute__((ext_vector_type(4)));
typedef float f32x16 __attribute__((ext_vector_type(16)));
typedef float f32x4 __attribute__((ext_vector_type(4)));
typedef float f32x2 __attribute__((ext_vector_type(2)));
typedef unsigned u32x4 __attribute__((ext_vector_type(4)));
typedef unsigned u32x2 __attribute__((ext_vector_type(2)));
typedef __bf16 bf2 __attribute__((ext_vector_type(2)));
#define LAS __attribute__((address_space(3)))
#define GAS __attribute__((address_space(1)))
#define DI __device__ __forceinline__
typedef GAS unsigned gu32;
typedef GAS unsigned long long gu64;
#define RLX_AGENT __ATOMIC_RELAXED, __HIP_MEMORY_SCOPE_AGENT
template <int V> using ic = std::integral_constant<int, V>;

constexpr int L = 2048, D = 576, F = 4608;
constexpr int NFFN = 12, NSL = 16, FS = 384, NCH = 5, NSIG = 32 * NCH;
constexpr int LDS_BYTES = 163840;
constexpr size_t PART_BLK = 9 * 8 * 2048;
constexpr size_t WS_PART = 0;
constexpr size_t WS_STAT = WS_PART + (size_t)NSL * 16 * PART_BLK;
constexpr size_t WS_EPOCH = WS_STAT + L * 8;
constexpr size_t WS_GRAN = WS_EPOCH + 1024;
constexpr size_t WS_REPOCH = WS_GRAN + 4 * 16 * 2 * NSIG * 8;
constexpr size_t WS_FLAG = WS_REPOCH + 16 * 256;
constexpr size_t WS_DBG = WS_FLAG + 16 * 16 * 256;
constexpr float BETA = 0.46866869f;
constexpr float KAP = 4.9122295f;
constexpr float IBETA = 1.0f / BETA;

#define SBAR() __builtin_amdgcn_sched_barrier(0)
#define PIN(x) asm volatile("" : "+v"(x))
#define MFMA(a, b, c) __builtin_amdgcn_mfma_f32_32x32x16_bf16(a, b, c, 0, 0, 0)

DI float lo2f(unsigned v) { return __uint_as_float(v << 16); }
DI float hi2f(unsigned v) { return __uint_as_float(v & 0xffff0000u); }
DI unsigned pk2(float a, float b) { bf2 r = {(__bf16)a, (__bf16)b}; return __builtin_bit_cast(unsigned, r); }
DI float rbf(float a) { return (float)(__bf16)a; }
DI void dma1(unsigned voff, const char* sb, unsigned dst) {
  asm volatile("s_mov_b32 m0, %2\n\ts_nop 0\n\tglobal_load_lds_dwordx4 %0, %1" :: "v"(voff), "s"(sb), "s"(dst) : "memory");
}
DI void dma1v(const char* src, unsigned dst) {
  asm volatile("s_mov_b32 m0, %1\n\ts_nop 0\n\tglobal_load_lds_dwordx4 %0, off" :: "v"(src), "s"(dst) : "memory");
}
template <int I, int N, class Fn> DI void sfor(Fn&& f) { if constexpr (I < N) { f(ic<I>{}); sfor<I + 1, N>(f); } }
template <int V, int G> DI void wait_bar() { SBAR(); asm volatile("s_waitcnt vmcnt(%0) lgkmcnt(%1)\n\ts_barrier" :: "n"(V), "n"(G) : "memory"); SBAR(); }
DI s16x4 trd(unsigned a) { return __builtin_amdgcn_ds_read_tr16_b64_v4i16((LAS s16x4*)a); }
DI bf16x8 cat(s16x4 lo, s16x4 hi) { return __builtin_shufflevector(lo, hi, 0, 1, 2, 3, 4, 5, 6, 7); }
DI u32x4 lds128(unsigned a) { return *(LAS u32x4*)a; }
DI void pst(unsigned voff, char* sb, u32x4 v) { asm volatile("global_store_dwordx4 %0, %1, %2 sc1\n\ts_nop 1" :: "v"(voff), "v"(v), "s"(sb) : "memory"); }
template <int OFF> DI void gload16(u32x4& d, unsigned voff, const char* sb) { asm volatile("global_load_dwordx4 %0, %1, %2 offset:%3" : "=v"(d) : "v"(voff), "s"(sb), "n"(OFF) : "memory"); }
template <int N> DI void wait6(u32x4* v) { asm volatile("s_waitcnt vmcnt(%6)" : "+v"(v[0]), "+v"(v[1]), "+v"(v[2]), "+v"(v[3]), "+v"(v[4]), "+v"(v[5]) : "n"(N) : "memory"); }
template <int N> DI void wait3(u32x4* v) { asm volatile("s_waitcnt vmcnt(%3)" : "+v"(v[0]), "+v"(v[1]), "+v"(v[2]) : "n"(N) : "memory"); }
DI void pin4(f32x2* t) { asm volatile("" : "+v"(t[0]), "+v"(t[1]), "+v"(t[2]), "+v"(t[3])); }
DI f32x4 mf4(u32x2 x, u32x2 y, f32x4 z) { return __builtin_amdgcn_mfma_f32_4x4x4bf16_1k(__builtin_bit_cast(s16x4, x), __builtin_bit_cast(s16x4, y), z, 0, 0, 0); }
DI void pin1(unsigned& v) { PIN(v); }
DI void stamp2(unsigned ad) { unsigned long long t; asm volatile("s_memtime %0\n\ts_waitcnt lgkmcnt(0)" : "=s"(t) :: "memory"); *(LAS unsigned*)ad = (unsigned)t; }
DI void dma1s(unsigned voff, const char* sb, unsigned dst) {
  asm volatile("s_mov_b32 m0, %2\n\ts_nop 0\n\tglobal_load_lds_dwordx4 %0, %1 sc1" :: "v"(voff), "s"(sb), "s"(dst) : "memory");
}
DI void gload8s(u32x2& d, unsigned voff, const char* sb) { asm volatile("global_load_dwordx2 %0, %1, %2 sc1" : "=v"(d) : "v"(voff), "s"(sb) : "memory"); }
DI void pin2(u32x2& v) { PIN(v); }
DI void flag_store(char* p, unsigned n, unsigned tag, int w) {
  u32x2 v = {n, tag}; unsigned z = 0; unsigned long long keep;
  asm volatile("s_cmp_lg_u32 %4, 0\n\ts_cbranch_scc1 1f\n\ts_mov_b64 %0, exec\n\ts_mov_b64 exec, 1\n\tglobal_store_dwordx2 %1, %2, %3 sc1\n\ts_mov_b64 exec, %0\n1:"
               : "=&s"(keep) : "v"(z), "v"(v), "s"(p), "s"(w) : "memory", "scc");
}
constexpr unsigned long long GMASK = 0xFFFF0000FFFF0000ull, HMASK = 0xFFFFFFFF00000000ull;
DI float selm(float a, float b, unsigned long long m) { float r; asm("v_cndmask_b32 %0, %1, %2, %3" : "=v"(r) : "v"(a), "v"(b), "s"(m)); return r; }
DI void touch(unsigned voff, const char* sb, unsigned dst) { asm volatile("s_mov_b32 m0, %2\n\ts_nop 0\n\tglobal_load_lds_dword %0, %1" :: "v"(voff), "s"(sb), "s"(dst) : "memory"); }
template <int OFF> DI void gload_hi(float& d, unsigned voff, const char* sb) { asm volatile("global_load_short_d16_hi %0, %1, %2 offset:%3" : "+v"(d) : "v"(voff), "s"(sb), "n"(OFF) : "memory"); }
#define OFL " sc1"
template <int OFF> DI void gstore4(unsigned voff, float* sb, float v) { asm volatile("global_store_dword %0, %1, %2 offset:%3" OFL :: "v"(voff), "v"(v), "s"(sb), "n"(OFF) : "memory"); }
DI void pinf(float& v) { PIN(v); }
template <int CTRL> DI float dpp(float v) { return __int_as_float(__builtin_amdgcn_update_dpp(0, __float_as_int(v), CTRL, 0xf, 0xf, true)); }
struct cf { float r, i; };
DI cf cmul(cf a, cf b) { return {__builtin_fmaf(a.r, b.r, -a.i * b.i), __builtin_fmaf(a.r, b.i, a.i * b.r)}; }
DI cf csq(cf a) { return {__builtin_fmaf(a.r, a.r, -a.i * a.i), (a.r + a.r) * a.i}; }
DI cf cmad(cf a, cf b, cf c) { return {__builtin_fmaf(a.r, b.r, __builtin_fmaf(-a.i, b.i, c.r)), __builtin_fmaf(a.r, b.i, __builtin_fmaf(a.i, b.r, c.i))}; }
DI void swap32(float x, float& lo, float& hi) { auto r = __builtin_amdgcn_permlane32_swap(__float_as_uint(x), __float_as_uint(x), false, false); lo = __uint_as_float(r[0]); hi = __uint_as_float(r[1]); }
DI void swap16(float x, float y, float& a, float& b) { auto r = __builtin_amdgcn_permlane16_swap(__float_as_uint(x), __float_as_uint(y), false, false); a = __uint_as_float(r[0]); b = __uint_as_float(r[1]); }

template <bool SSM> struct Cfg {
  static constexpr int NP = SSM ? 18 * NCH : 108, NP1 = NP / 2, NM = 4 * NP, NM1 = NM / 2, NI = NP;
  static constexpr int PITCH = SSM ? 8448 : 8192, NSLOT = SSM ? 18 : 19;
  static constexpr int UNIT2 = SSM ? 4 * NCH : 24;
  static constexpr int MISC = NSLOT * PITCH;
};
constexpr bool bar_at(int m) { return m % 12 == 0 || (m < 36 && m % 4 == 0); }
constexpr int next_bar(int m) { int b = m + 1; while (!bar_at(b)) ++b; return b; }
constexpr int item_piece(int q) { return q; }
constexpr int witem(int p) { return p; }
constexpr int pslot(int ch) { return ch == 0 ? 17 : ch == 1 ? 15 : ch == 2 ? 13 : ch == 3 ? 11 : ch == 4 ? 17 : 15; }
constexpr int HSLOT = 8, KSLOT = 14;
template <bool SSM> constexpr int p2_dp(int j) { return SSM ? j / NCH : (j < 12 ? (j & 1) : j / 6); }
template <bool SSM> constexpr int p2_ch(int j) { return SSM ? j % NCH : (j < 12 ? (j >> 1) : j % 6); }
constexpr int h_ch(int n) { return n >= 16 ? 4 : 2 * (n >> 3) + ((n >> 1) & 1); }
constexpr int h_kk(int n) { return n >= 16 ? n & 3 : 2 * ((n >> 2) & 1) + (n & 1); }
template <bool SSM> constexpr int store_of(int m) {
  using C = Cfg<SSM>; const int m2 = m - C::NM1;
  if (m2 < 0) return -1;
  if (!SSM && m2 < 72) return m2 == 50 ? 0 : m2 == 54 ? 1 : m2 == 58 ? 2 : m2 == 62 ? 3 : -1;
  if (m2 < C::UNIT2) return -1;
  return m2 % C::UNIT2 == 4 ? 2 * (m2 / C::UNIT2 - 1) : m2 % C::UNIT2 == 8 ? 2 * (m2 / C::UNIT2 - 1) + 1 : -1;
}
constexpr int NPF = 9;
constexpr int pf_of(int m) { return (m >= 48 && m < 48 + 12 * NPF && m % 12 == 0) ? (m - 48) / 12 : -1; }
#define NPF1 0
constexpr int pf1_of(int m) { return (m >= PF1S && m < PF1S + 4 * NPF1 && m % 4 == 0) ? (m - PF1S) / 4 : -1; }
constexpr int I0 = 6, LOOK = 18, NXP = 6;
constexpr int xslot(int p) { return p < NXP ? 2 * p : 2 * p - 8; }
constexpr int JORD[18] = {0, 2, 4, 6, 1, 3, 5, 7, 8, 9, 10, 11, 12, 13, 14, 15, 16, 17};
#define JV 0
constexpr int JC[10] = {0, 0, 0, 2, 2, 3, 0, 0, 0, 0}, N0[10] = {0, 0, 0, 0, 0, 4, 0, 0, 0, 0}, N1[10] = {0, 0, 0, 0, 0, 3, 0, 0, 0, 0}, FD[10] = {0, 0, 0, 0, 0, 10, 0, 0, 0, 0};
constexpr int JE0 = 5, MAXPUB = 6;
constexpr int jcount(int e) { return e >= 0 && e < 10 ? JC[e] : 0; }
constexpr int jfirst(int e) { int n = 0; for (int k = 9; k > e; --k) n += JC[k]; return n; }
constexpr int NJIN = jfirst(0);
template <bool SSM> constexpr int last_store(int dp) { for (int m = Cfg<SSM>::NM - 1; m >= 0; --m) if (store_of<SSM>(m) == 2 * dp + 1) return m; return 100000; }
template <bool SSM> constexpr int pub_count(int B) { int n = 0;
  while (n < MAXPUB && last_store<SSM>(n) + (last_store<SSM>(n) < Cfg<SSM>::NM - 12 * JE0 ? 14 : SSM ? 20 : 26) <= B) ++n; return n; }
template <bool SSM> constexpr bool pub_at(int B) { return B % 12 == 0 && pub_count<SSM>(B) >= 3 && pub_count<SSM>(B) != pub_count<SSM>(B - 12); }
template <bool SSM> constexpr int chk_at(int B) { const int e = (Cfg<SSM>::NM - B) / 12; return (B % 12 == 0 && e > 0 && e < 10 && FD[e]) ? e : 0; }
template <bool SSM> constexpr int fld_of(int B) { return B - FD[chk_at<SSM>(B)]; }
template <bool SSM> constexpr bool fld_at(int m) { for (int e = 1; e < 10; ++e) if (FD[e] && Cfg<SSM>::NM - 12 * e - FD[e] == m) return true; return false; }
template <bool SSM> constexpr int job_at(int m) {
  const int e = (Cfg<SSM>::NM - (m - m % 12)) / 12, k = (m % 12) / 4; return (m % 4 == 2 && k < jcount(e)) ? jfirst(e) + k : -1; }
template <bool SSM> constexpr int xld_at(int m) { return -1; }
struct Sched { int slot[126]; int istep[126]; int vm[432]; int nis[432]; int fis[432]; int jslot[18]; bool ok; };
template <bool SSM> constexpr Sched make_sched() {
  using C = Cfg<SSM>;
  Sched s{};
  int free_at[19] = {}, b0[19] = {}, b1[19] = {};
  for (int i = 0; i < 19; ++i) { free_at[i] = i < 2 * NXP ? ((i >= 2 * NXP - 8 && i < 10) ? 20 + 4 * (i >> 1) : 4 + 4 * (i >> 1)) : -1; b0[i] = 1000; b1[i] = 1000; }
  if (SSM) { for (int i = HSLOT; i < 18; ++i) { b0[i] = i >= KSLOT ? 72 : C::NM1; b1[i] = C::NM1 + (i < HSLOT + 7 ? 0 : 12); } }
  else { b0[17] = b0[18] = 72; b1[17] = b1[18] = 240; b0[15] = b0[16] = 108; b1[15] = b1[16] = 252; b0[13] = b0[14] = 144; b1[13] = b1[14] = 204; b0[11] = b0[12] = 180; b1[11] = b1[12] = 216; }
  int q = 0; bool jok = true;
  for (int B = -1; B < C::NM; B = B < 0 ? 0 : next_bar(B)) {
    const int len = B < 0 ? I0 : next_bar(B) - B, maxi = B < 0 ? I0 : len == 4 ? 1 + ((B >> 2) & 1) : 4;
    if (B >= 36) for (int k = 0; k < jcount((C::NM - B) / 12); ++k) {
      int best = -1; for (int i = 0; i < C::NSLOT; ++i) if (free_at[i] <= B) { best = i; break; }
      if (best < 0) jok = false; else { s.jslot[JORD[jfirst((C::NM - B) / 12) + k]] = best; free_at[best] = 100000; } }
    int n = 0;
    while (n < maxi && q + n < C::NI && item_piece(q + n) < (B < 0 ? 0 : B) / 4 + 18) {
      const int it = q + n, end = next_bar(4 * item_piece(it) + 3); int best = -1;
      int key = 100000;
      for (int i = C::NSLOT - 1; i >= 0; --i) if (free_at[i] <= B && (end <= b0[i] || B >= b1[i]) && !(SSM && it >= C::NP1 - 14 && it < C::NP1 && (i >= HSLOT) != (it >= C::NP1 - 6))) { const int k2 = B >= b1[i] ? 1000 : b0[i]; if (k2 < key) { key = k2; best = i; } }
      if (best < 0) break;
      s.slot[it] = best; free_at[best] = end; ++n;
    }
    for (int k = 0; k < n; ++k) s.istep[q + k] = B < 0 ? -1 : len == 4 ? B + 2 * k + 1 : B + (n <= 3 ? 4 * k + 3 : 3 * k + 1);
    q += n;
  }
  for (int k = NJIN; k < 18; ++k) { int best = -1; for (int i = 0; i < C::NSLOT; ++i) if (free_at[i] <= C::NM) { best = i; break; }
    if (best < 0) jok = false; else { s.jslot[JORD[k]] = best; free_at[best] = 100000; } }
  s.ok = q == C::NI && jok;
  for (int m = 0; m < C::NM; ++m) { s.nis[m] = 0; s.fis[m] = 0; }
  for (int it = C::NI - 1; it >= 0; --it) if (s.istep[it] >= 0) { s.nis[s.istep[it]]++; s.fis[s.istep[it]] = it; }
  for (int B = 0; B < C::NM; B = next_bar(B)) {
    int np = next_bar(B) / 4; if (np > C::NP - 1) np = C::NP - 1;
    const int need = witem(np);
    if (s.istep[need] >= B) { s.ok = false; continue; }
    int cnt = 0;
    for (int it = need + 1; it < C::NI; ++it) if (s.istep[it] < B) ++cnt;
    for (int m = s.istep[need] + 1; m < B; ++m) if (m >= 0) cnt += (store_of<SSM>(m) >= 0) + (pf_of(m) >= 0) + (pf1_of(m) >= 0) + fld_at<SSM>(m) + (job_at<SSM>(m) >= 0) + (xld_at<SSM>(m) >= 0);
    for (int t = 0; t < 2; ++t) {
      const int m0 = t == 0 ? (pub_at<SSM>(B) ? last_store<SSM>(pub_count<SSM>(B) - 1) : -1) : (chk_at<SSM>(B) ? fld_of<SSM>(B) : -1);
      if (m0 < 0) continue;
      int c2 = (t == 0 ? fld_at<SSM>(m0) : 0) + (job_at<SSM>(m0) >= 0) + s.nis[m0];
      for (int m = m0 + 1; m < B; ++m) c2 += (store_of<SSM>(m) >= 0) + fld_at<SSM>(m) + (job_at<SSM>(m) >= 0) + (xld_at<SSM>(m) >= 0) + s.nis[m];
      if (c2 < cnt) cnt = c2;
    }
    s.vm[B] = cnt; if (cnt > 63) s.ok = false;
  }
  return s;
}
template <bool SSM> struct SC { static constexpr Sched s = make_sched<SSM>(); };
static_assert(SC<false>::s.ok && SC<true>::s.ok, "bad schedule");

#define TS(i)

struct Args {
  const u16 *x, *lre, *lim, *bre, *bim, *cre, *cim, *ldt, *dsk, *w1, *b1, *w2, *b2;
  char* ws; float* out;
};

typedef const __attribute__((address_space(4))) Args* KArgs;
DI KArgs kargs() { KArgs p = (KArgs)__builtin_amdgcn_kernarg_segment_ptr(); asm volatile("" : "+s"(p)); return p; }
template <bool SSM> DI void body(const unsigned lds0, const int slice, const int rb) {
  using C = Cfg<SSM>;
#define S SC<SSM>::s
  Args a;
  { KArgs k = kargs(); a.x = k->x; a.ws = k->ws;
    if constexpr (SSM) { a.lre = k->lre; a.lim = k->lim; a.ldt = k->ldt; a.bre = k->bre; a.bim = k->bim; a.cre = k->cre; a.cim = k->cim; }
    else { a.w1 = k->w1; a.b1 = k->b1; a.w2 = k->w2; }
    a.b2 = k->b2; a.dsk = k->dsk;
    if constexpr (SSM) asm volatile("" : "+s"(a.lre), "+s"(a.lim), "+s"(a.ldt), "+s"(a.b2), "+s"(a.dsk)); else asm volatile("" : "+s"(a.b1), "+s"(a.b2), "+s"(a.dsk)); }
  constexpr int PITCH = C::PITCH, TAB = C::MISC, TAB1 = TAB + NSIG * 16, SEG = TAB1 + NSIG * 16, FST = LDS_BYTES - 512, ETAB = FST - 576, CSX = ETAB - 320;
  static_assert(!SSM || SEG + 4 * NSIG * 8 <= CSX, "");
  int tid = threadIdx.x, lane = tid & 63;
  const int w = __builtin_amdgcn_readfirstlane(tid >> 6), r = w & 3, c = w >> 2;
  TS(0);
  const int f0 = slice * FS, t0 = rb * 128;
  const int sg = slice - NFFN, p0 = 128 * sg, p4 = 512 + 32 * (sg & 1);
  int h = lane >> 5, l31 = lane & 31;
  const int drow = 8 * w + (lane >> 3), dcp = lane & 7;
  const unsigned voffX = (lane >> 3) * 1152 + ((dcp ^ ((drow >> 1) & 7)) << 4);
  const int xrot = ROT * (slice >> 2);
  const int xblk[2] = {(w + xrot) & 15, (w + 8 + xrot) & 15};
  const char* xb = (const char*)(a.x + (size_t)t0 * D);
  const unsigned ddst = lds0 + w * (SSM ? 1056 : 1024);
  unsigned voff1, voff2; const char *sb1, *sb2;
  if constexpr (SSM) {
    const int sw = dcp ^ ((drow >> 1) & 1);
    voff1 = (16 * c + 8 * (w & 1) + (lane >> 3)) * 1152 + (sw << 4);
    sb1 = (const char*)(((w >> 1) & 1) ? a.bim : a.bre);
    sb2 = (const char*)(c ? a.cim : a.cre);
  } else {
    const unsigned sw = (dcp ^ (((drow >> 1) & 1) << 2)) << 4;
    voff1 = drow * 9216 + sw; voff2 = drow * 1152 + sw;
    sb1 = (const char*)(a.w1 + f0); sb2 = (const char*)(a.w2 + (size_t)f0 * D);
  }
  auto issue = [&](auto it_) {
    constexpr int p = it_, q = p - C::NP1, so = S.slot[p] * PITCH;
    if constexpr (SSM) {
      if constexpr (p < C::NP1) dma1(voff1, sb1 + (p / 9 < 4 ? p0 + (p / 9) * 32 : p4) * 1152 + (p % 9) * 128, ddst + so);
      else {
        if constexpr (q == 0) { int tz = threadIdx.x; PIN(tz); const int dr = (tz >> 2) & 63, d8 = tz >> 3;
          voff2 = dr * 1152 + (((tz & 3) ^ ((dr >> 3) & 1)) << 4); voff1 = d8 * 1152 + (((tz & 7) ^ ((d8 >> 1) & 1)) << 4); }
        constexpr int dp = q / NCH, j = q % NCH;
        if constexpr (j < 4) dma1(voff1, (const char*)((j & 1) ? a.cim : a.cre) + dp * (64 * 1152) + p0 * 2 + (j >> 1) * 128, ddst + so);
        else dma1(voff2, sb2 + dp * (64 * 1152) + p4 * 2, ddst + so);
      }
    } else {
      if constexpr (p < C::NP1) dma1(voff1, sb1 + (p / 9) * 128 + (size_t)(p % 9) * (64 * 9216), ddst + so);
      else dma1(voff2, sb2 + p2_dp<SSM>(q) * 128 + p2_ch<SSM>(q) * (64 * 1152), ddst + so);
    }
  };
  const int jd0 = (slice >> 3) * 32 + (slice & 1) * 16, jt0 = ((slice >> 1) & 3) * 32;
  unsigned rtag = *(const __attribute__((address_space(4))) unsigned*)(a.ws + WS_REPOCH + rb * 256) + 1u;
  unsigned tag = 0;
  if constexpr (SSM) tag = *(const __attribute__((address_space(4))) unsigned*)(a.ws + WS_EPOCH + sg * 256) + 1u;
  u32x2 braw; unsigned plr, pli, pdt, eb2, edk;
  if (w < 3) {
    if constexpr (SSM) {
      const int sp = tid < 128 ? p0 + tid : p4 + (tid & 31);
      asm volatile("global_load_ushort %0, %1, off" : "=v"(plr) : "v"(a.lre + sp) : "memory");
      asm volatile("global_load_ushort %0, %1, off" : "=v"(pli) : "v"(a.lim + sp) : "memory");
      asm volatile("global_load_ushort %0, %1, off" : "=v"(pdt) : "v"(a.ldt + sp) : "memory");
    } else {
      asm volatile("global_load_dwordx2 %0, %1, off" : "=v"(braw) : "v"(a.b1 + f0 + 4 * (tid < 96 ? tid : 95)) : "memory");
    }
    const int ti = min(tid, 143), d = (ti >> 4) * 64 + jd0 + (ti & 15);
    asm volatile("global_load_ushort %0, %1, off" : "=v"(eb2) : "v"(a.b2 + d) : "memory");
    asm volatile("global_load_ushort %0, %1, off" : "=v"(edk) : "v"(a.dsk + d) : "memory");
  } else if (w == 3) {
    const char* pc; asm volatile("s_getpc_b64 %0" : "=s"(pc));
    touch(min(lane, CODEPF / 1024 - 1) * 128, pc + (rb & 7) * (CODEPF / 8), lds0 + LDS_BYTES - 256);
  }
  u32x4 xr[18];
  sfor<0, 9>([&](auto k_) { gload16<k_ * 128>(xr[k_], voffX, xb + xblk[0] * 9216); });
  u32x4 xf[36];
  unsigned xa[2][4];
  {
    const int xrow = 32 * (r & 1) + (SSM ? 16 * ((l31 >> 2) & 1) + (l31 & 3) + 4 * (l31 >> 3) : l31), xkey = (xrow >> 1) & 7;
#pragma unroll
    for (int kk = 0; kk < 4; ++kk) { xa[0][kk] = lds0 + (r >> 1) * PITCH + xrow * 128 + (((2 * kk + h) ^ xkey) << 4); xa[1][kk] = xa[0][kk] + 8 * PITCH; PIN(xa[0][kk]); PIN(xa[1][kk]); } }
  unsigned fb1[3], fb2[3];
  if constexpr (SSM) {
    const int row = 32 * c + l31;
#pragma unroll
    for (int i = 0; i < 3; ++i) fb1[i] = lds0 + (row >> 3) * 1056 + (row & 7) * 128 + ((h ^ ((row >> 1) & 1)) << 4) + i * 65536;
  } else {
    const int q4 = (lane & 15) >> 2, pq = lane & 3, cb = (lane >> 4) & 1;
    const unsigned colb = (((c * 4 + cb * 2 + (pq >> 1)) ^ ((q4 >> 1) << 2)) << 4) + 8 * (pq & 1);
#pragma unroll
    for (int i = 0; i < 3; ++i) { fb1[i] = lds0 + (8 * h + q4) * 128 + colb + i * 65536; fb2[i] = lds0 + (4 * h + q4) * 128 + colb + i * 65536; }
  }
  PIN(fb1[0]); PIN(fb1[1]); PIN(fb1[2]); PIN(voff1);
  auto fread = [&](auto m_) -> bf16x8 {
    constexpr int p = m_ / 4, kk = m_ & 3;
    constexpr bool mixed = SSM && p >= C::NP1 && (p - C::NP1) % NCH == 4;
    constexpr int off = S.slot[p] * PITCH + (!SSM ? kk * 2048 : !mixed ? kk * 32 : (kk >> 1) * 4224 + (kk & 1) * 32);
    if constexpr (SSM && m_ == C::NM1 + 16) { int tz = threadIdx.x; PIN(tz); const int row = 32 * c + (tz & 31);
#pragma unroll
      for (int i = 0; i < 3; ++i) { fb2[i] = lds0 + (row >> 4) * 1056 + (row & 15) * 64 + ((((tz >> 5) ^ (row >> 3)) & 1) << 4) + i * 65536; pin1(fb2[i]); } }
    if constexpr (mixed) return __builtin_bit_cast(bf16x8, lds128(fb2[off >> 16] + (off & 65535)));
    else if constexpr (SSM) return __builtin_bit_cast(bf16x8, lds128(fb1[off >> 16] + (off & 65535)));
    else if constexpr (p < C::NP1) return cat(trd(fb1[off >> 16] + (off & 65535)), trd(fb1[off >> 16] + (off & 65535) + 512));
    else return cat(trd(fb2[off >> 16] + (off & 65535)), trd(fb2[off >> 16] + (off & 65535) + 1024)); };
  auto xread = [&](int m) { const int sl = xslot(m >> 2); xf[m] = lds128(xa[sl >> 3][m & 3] + (sl & 7) * PITCH); };
  unsigned pwr, prd, biasb;
  if constexpr (!SSM) { pwr = lds0 + w * 2048 + lane * 16 + 11 * PITCH; prd = lds0 + r * 2048 + lane * 16 + 11 * PITCH; PIN(pwr);
    biasb = lds0 + TAB + (32 * c + 4 * h) * 4; PIN(biasb); }
  TS(1);
  unsigned xw[2][2];
#pragma unroll
  for (int i = 0; i < 2; ++i) { xw[i][0] = lds0 + (xblk[i] & 7) * 1024 + (xblk[i] >> 3) * PITCH + lane * 16; xw[i][1] = xw[i][0] + 6 * PITCH; PIN(xw[i][0]); PIN(xw[i][1]); }
  auto xput = [&](int k) { const int sl = xslot(k % 9); *(LAS u32x4*)(xw[k / 9][sl / 6] + (sl % 6) * PITCH) = xr[k]; };
  u32x2 ones = {0x3f803f80u, 0x3f803f80u}; PIN(ones);
  sfor<0, 2>([&](auto hf_) { constexpr int hf = hf_;
    f32x4 sa[2] = {}, sq[2] = {};
    sfor<0, 3>([&](auto g_) { constexpr int k0 = 9 * hf + 3 * g_;
      wait3<(hf ? 15 - k0 + I0 : 6)>(xr + k0);
      if constexpr (hf == 0) sfor<k0, k0 + 3>([&](auto k_) { gload16<k_ * 128>(xr[9 + k_], voffX, xb + xblk[1] * 9216); });
#pragma unroll
      for (int k = k0; k < k0 + 3; ++k)
#pragma unroll
        for (int e = 0; e < 2; ++e) { const u32x2 v = {xr[k][2 * e], xr[k][2 * e + 1]}; sa[e] = mf4(ones, v, sa[e]); sq[e] = mf4(v, v, sq[e]); } });
    if constexpr (hf == 0) { TS(2); } else { TS(14); }
    const f32x4 q4 = sq[0] + sq[1];
    float d0 = q4[0], d1 = q4[1], d2 = q4[2], d3 = q4[3]; PIN(d1); PIN(d3);
    d0 = (lane & 1) ? d1 : d0; d2 = (lane & 1) ? d3 : d2;
    float s1 = sa[0][0] + sa[1][0], s2 = (lane & 2) ? d2 : d0;
    s1 += dpp<0xB1>(s1); s2 += dpp<0xB1>(s2); s1 += dpp<0x4E>(s1); s2 += dpp<0x4E>(s2); s1 += dpp<0x141>(s1); s2 += dpp<0x141>(s2);
    const float mean = s1 * (1.f / D);
    const float var = fmaxf(s2 * (1.f / D) - mean * mean, 0.f);
    const float mb = rbf(mean), iden = __builtin_amdgcn_rcpf(rbf(__builtin_amdgcn_sqrtf(rbf(rbf(var) + 1e-5f))));
    const float inv = (SSM ? 1.f : BETA) * iden, ih = rbf(inv), il = rbf(inv - ih), nb = -mb * (ih + il);
    if ((xblk[hf] >> 2) == (jt0 >> 5)) *(LAS f32x2*)(lds0 + FST + ((xblk[hf] & 3) * 8 + (lane >> 3)) * 8) = (f32x2){mb, iden};
    const unsigned bh = __float_as_uint(ih) >> ((lane & 1) ? 0 : 16), bl = __float_as_uint(il) >> ((lane & 1) ? 0 : 16);
    const u32x2 Ah = {(lane & 2) ? 0u : bh, (lane & 2) ? bh : 0u}, Al = {(lane & 2) ? 0u : bl, (lane & 2) ? bl : 0u};
    const f32x4 nv = {nb, nb, nb, nb};
#pragma unroll
    for (int k3 = 9 * hf; k3 < 9 * hf + 9; k3 += 3) {
      f32x4 t[6];
#pragma unroll
      for (int e = 0; e < 6; ++e) t[e] = mf4(Al, (u32x2){xr[k3 + (e >> 1)][2 * (e & 1)], xr[k3 + (e >> 1)][2 * (e & 1) + 1]}, nv);
#pragma unroll
      for (int e = 0; e < 6; ++e) t[e] = mf4(Ah, (u32x2){xr[k3 + (e >> 1)][2 * (e & 1)], xr[k3 + (e >> 1)][2 * (e & 1) + 1]}, t[e]);
#pragma unroll
      for (int e = 0; e < 6; ++e) { xr[k3 + (e >> 1)][2 * (e & 1)] = pk2(t[e][0], t[e][1]); xr[k3 + (e >> 1)][2 * (e & 1) + 1] = pk2(t[e][2], t[e][3]); }
#pragma unroll
      for (int k = k3; k < k3 + 3; ++k) if (k % 9 < NXP) xput(k);
    }
    if constexpr (hf == 0) sfor<0, I0>(issue);
  });
  if (w < 3) {
    PIN(eb2); PIN(edk);
    *(LAS unsigned*)(lds0 + ETAB + min(tid, 143) * 4) = eb2 | (edk << 16);
    if constexpr (SSM) {
      PIN(plr); PIN(pli); PIN(pdt);
      if (tid < NSIG) {
        const float lr = lo2f(plr), li = lo2f(pli), dt = rbf(expf(lo2f(pdt)));
        const float e = expf(dt * lr); float sn, cs; sincosf(dt * li, &sn, &cs);
        const float ar = e * cs, ai = e * sn, nr = ar - 1.f, den = lr * lr + li * li;
        const float k2 = (sg >= 2 && tid >= 128) ? 0.f : 2.f / den;
        *(LAS f32x4*)(lds0 + TAB + tid * 16) = (f32x4){ar, ai, (nr * lr + ai * li) * k2, (ai * lr - nr * li) * k2};
        const cf a8 = csq(csq(csq(cf{ar, ai}))), a16 = csq(a8);
        *(LAS f32x4*)(lds0 + TAB1 + tid * 16) = (f32x4){a8.r, a8.i, a16.r, a16.i};
      }
    } else {
      PIN(braw);
      if (tid < 96) *(LAS f32x4*)(lds0 + TAB + tid * 16) = (f32x4){BETA * lo2f(braw[0]), BETA * hi2f(braw[0]), BETA * lo2f(braw[1]), BETA * hi2f(braw[1])};
    }
  }
  asm volatile("" : "+s"(rtag), "+s"(tag));
  TS(15);
  wait_bar<S.vm[0], 0>();
  TS(3);
  bf16x8 af[4];
  f32x16 acc, prev, oacc[9];
  u32x4 P[6][4], pn[2], nbias[4];
  af[0] = fread(ic<0>{}); xread(0);
  if constexpr (!SSM) {
#pragma unroll
    for (int g = 0; g < 4; ++g) nbias[g] = lds128(biasb + g * 32); }
  af[1] = fread(ic<1>{}); xread(1); af[2] = fread(ic<2>{}); xread(2);
  cf u[NCH][8];
  const f32x16 zero16 = {};
  float gt0, gt1;
  char* pbase; unsigned pvo, jvo;
  auto gelu_ph = [&](int e, int ph) {
    const float v0 = prev[2 * e], v1 = prev[2 * e + 1];
    if (ph == 0) { gt0 = v0 * __builtin_fmaf(v0, v0, KAP); gt1 = v1 * __builtin_fmaf(v1, v1, KAP); PIN(gt0); PIN(gt1); }
    if (ph == 1) { gt0 = __builtin_amdgcn_exp2f(-gt0); gt1 = __builtin_amdgcn_exp2f(-gt1); PIN(gt0); PIN(gt1); }
    if (ph == 2) { gt0 = __builtin_amdgcn_rcpf(1.f + gt0); gt1 = 1.f + gt1; PIN(gt0); PIN(gt1); }
    if (ph == 3) { unsigned pv = pk2(v0 * gt0, v1 * __builtin_amdgcn_rcpf(gt1)); PIN(pv); pn[e >> 2][e & 3] = pv; }
  };
  cf sE, sH, sU;
  f32x4 stab, stab1;
  unsigned segw;
#define PINC(z) do { if (pin) { PIN((z).r); PIN((z).i); } } while (0)
  auto pass1 = [&](int ch, int k, bool pin) {
    if (k == 0) { int tz = threadIdx.x; PIN(tz); segw = lds0 + (32 * ch + 16 * c + (tz & 15)) * 8;
      stab = *(LAS f32x4*)(2 * segw - lds0 + TAB); }
    const cf av = {stab[0], stab[1]};
    if (k >= 1 && k <= 4) { for (int j = 2 * k - 2; j < 2 * k; ++j) { swap16(prev[j], prev[j + 8], u[ch][j].r, u[ch][j].i); PINC(u[ch][j]); } }
    if (k >= 5 && k <= 11) { const int i = k - 5; sE = cmad(av, i ? sE : u[ch][0], u[ch][i + 1]); PINC(sE); }
    if (k == 9) stab1 = *(LAS f32x4*)(2 * segw - lds0 + TAB1);
    const cf a8 = {stab1[0], stab1[1]}, a16 = {stab1[2], stab1[3]};
    if (k == 12) { swap16(sE.r, sE.r, sE.r, sH.r); swap16(sE.i, sE.i, sE.i, sH.i); PINC(sH); PINC(sE); }
    if (k == 13) { sU = cmad(a8, sE, sH); PINC(sU); }
    if (k == 14) { swap32(sU.r, sU.r, sH.r); swap32(sU.i, sU.i, sH.i); PINC(sH); PINC(sU); }
    if (k == 15) { cf Sg = cmad(a16, sU, sH); PINC(Sg); *(LAS f32x2*)(segw + SEG + r * NSIG * 8) = (f32x2){Sg.r, Sg.i}; }
    if (k == 16) { sU = {selm(0.f, sU.r, HMASK), selm(0.f, sU.i, HMASK)}; sE = cmad(a8, sU, sE); PINC(sU); PINC(sE); }
    if (k == 17) { u[ch][0] = cmad(av, cf{selm(sU.r, sE.r, GMASK), selm(sU.i, sE.i, GMASK)}, u[ch][0]); PINC(u[ch][0]); }
    if (k >= 18) { const int j = k - 18; u[ch][j] = cmul(cf{stab[2], stab[3]}, u[ch][j]); PINC(u[ch][j]); }
  };
  char* const granb = a.ws + WS_GRAN + (size_t)(sg * 16 + 15 - rb) * (NSIG * 16);
  auto seg_rd = [&](int t, cf* sv) {
#pragma unroll
    for (int i = 0; i < 4; ++i) { const f32x2 v = *(LAS f32x2*)(lds0 + SEG + (i * NSIG + t) * 8); sv[i] = {v[0], v[1]}; } };
  auto pow32 = [&](int t) { const f32x2 v = *(LAS f32x2*)(lds0 + TAB1 + t * 16 + 8); return csq(cf{v[0], v[1]}); };
  auto carry_out = [&](int t, cf a32) { cf sv[4]; seg_rd(t, sv);
    const cf T = cmad(a32, cmad(a32, cmad(a32, sv[0], sv[1]), sv[2]), sv[3]);
    __builtin_amdgcn_raw_buffer_store_b128((u32x4){__float_as_uint(T.r), tag, __float_as_uint(T.i), tag}, __builtin_amdgcn_make_buffer_rsrc(granb, 0, NSIG * 16, 0x00020000), t * 16, 0, 16); };
  auto carry_ld = [&](int t, u32x4* v) {
    const auto rs = __builtin_amdgcn_make_buffer_rsrc(granb + NSIG * 16, 0, rb * (NSIG * 16), 0x00020000);
    unsigned vo = t * 16; PIN(vo);
#pragma unroll
    for (int k = 0; k < 15; ++k) v[k] = __builtin_amdgcn_raw_buffer_load_b128(rs, vo + k * (NSIG * 16), 0, 16); };
  auto carry_in = [&](int t, cf a32, u32x4* v) {
    cf sv[4]; seg_rd(t, sv);
    const cf a128 = csq(csq(a32)), a512 = csq(csq(a128));
    for (unsigned spins = 0; spins < (1u << 16); ++spins) {
      unsigned sum = 0;
#pragma unroll
      for (int k = 0; k < 15; ++k) sum += v[k][1] + v[k][3];
      if (__all(sum == 2u * rb * tag)) break;
      carry_ld(t, v);
    }
    cf q[4];
#pragma unroll
    for (int i = 3; i >= 0; --i)
#pragma unroll
      for (int n = 0; n < 4; ++n) { const int k = 4 * n + i; if (k > 14) continue;
        const cf x = {__uint_as_float(v[k][0]), __uint_as_float(v[k][2])}; q[n] = (i == 3 || k == 14) ? x : cmad(a128, q[n], x); }
    cf G = cmad(a512, cmad(a512, cmad(a512, q[3], q[2]), q[1]), q[0]);
#pragma unroll
    for (int i = 0; i < 4; ++i) { *(LAS f32x2*)(lds0 + SEG + (i * NSIG + t) * 8) = (f32x2){G.r, G.i}; G = cmad(a32, G, sv[i]); } };
  unsigned hr[2][2];
  auto hread = [&](int n) { const int ch = h_ch(n), kk = h_kk(n); P[ch][kk] = __builtin_bit_cast(u32x4, cat(trd(hr[ch >> 2][0] + (ch & 3) * 16384 + kk * 1024), trd(hr[ch >> 2][1] + (ch & 3) * 16384 + kk * 1024))); };
  auto ssm_boundary = [&]() {
    tid = threadIdx.x; PIN(tid); lane = tid & 63; h = lane >> 5;
    const int ts = min(tid, NSIG - 1);
    u32x4 v[15];
    if (w < 2) carry_ld(ts, v);
    const cf a32 = pow32(ts);
#pragma unroll
    for (int k = 0; k < 16; ++k) pass1(NCH - 1, k, false);
    const int g = (lane >> 4) & 1, s16 = lane & 15;
#pragma unroll
    for (int k = 0; k < 4; ++k) { const f32x4 t = *(LAS f32x4*)(lds0 + KSLOT * PITCH + tid * 16 + k * 8192); u[0][2 * k] = {t[0], t[1]}; u[0][2 * k + 1] = {t[2], t[3]}; }
    SBAR();
    if (w < 2) carry_in(ts, a32, v);
    wait_bar<63, 0>();
    TS(8);
    if (w == 2) carry_out(ts, a32);
    const int q4 = (lane & 15) >> 2, pq = lane & 3;
    unsigned hw[2];
    hw[0] = lds0 + HSLOT * PITCH + r * 4096 + (16 * c + s16) * 64 + (((2 * h + g) ^ ((s16 >> 1) & 3)) << 4); hw[1] = hw[0] + 65536;
    PIN(hw[0]); PIN(hw[1]);
#pragma unroll
    for (int k = 0; k < 2; ++k) {
      hr[0][k] = lds0 + HSLOT * PITCH + r * 4096 + (8 * h + 4 * k + q4) * 64 + (((2 * g + (pq >> 1)) ^ (2 * k + (q4 >> 1))) << 4) + (pq & 1) * 8; hr[1][k] = hr[0][k] + 65536;
      PIN(hr[0][k]); PIN(hr[1][k]); }
    cf av[NCH], pw[NCH];
    auto prep = [&](int ch) {
      const f32x4 t = *(LAS f32x4*)(lds0 + TAB + (32 * ch + 16 * c + s16) * 16), t1 = *(LAS f32x4*)(lds0 + TAB1 + (32 * ch + 16 * c + s16) * 16);
      av[ch] = {t[0], t[1]};
      pw[ch] = cmul(cmul(cf{t[2], t[3]}, cf{selm(1.f, t1[0], GMASK), selm(0.f, t1[1], GMASK)}), cf{selm(1.f, t1[2], HMASK), selm(0.f, t1[3], HMASK)}); };
    auto fin = [&](int c0, int c1) {
      f32x2 z[NCH], o[NCH][8];
#pragma unroll
      for (int ch = c0; ch < c1; ++ch) { const f32x2 ci = *(LAS f32x2*)(lds0 + SEG + (r * NSIG + 32 * ch + 16 * c + s16) * 8);
        const cf z0 = cmul(pw[ch], cf{ci[0], ci[1]}); z[ch] = (f32x2){z0.r, z0.i}; }
#pragma unroll
      for (int i = 0; i < 8; ++i)
#pragma unroll
        for (int ch = c0; ch < c1; ++ch) {
          const f32x2 t = __builtin_elementwise_fma((f32x2){av[ch].r, av[ch].r}, z[ch], (f32x2){u[ch][i].r, u[ch][i].i});
          z[ch] = __builtin_elementwise_fma((f32x2){-av[ch].i, av[ch].i}, __builtin_shufflevector(z[ch], z[ch], 1, 0), t); o[ch][i] = z[ch]; }
#pragma unroll
      for (int ch = c0; ch < c1; ++ch) {
        *(LAS u32x4*)(hw[ch >> 2] + (ch & 3) * 16384) = (u32x4){pk2(o[ch][0][0], o[ch][1][0]), pk2(o[ch][2][0], o[ch][3][0]), pk2(o[ch][4][0], o[ch][5][0]), pk2(o[ch][6][0], o[ch][7][0])};
        *(LAS u32x4*)(hw[ch >> 2] + (ch & 3) * 16384 + 2048) = (u32x4){pk2(-o[ch][0][1], -o[ch][1][1]), pk2(-o[ch][2][1], -o[ch][3][1]), pk2(-o[ch][4][1], -o[ch][5][1]), pk2(-o[ch][6][1], -o[ch][7][1])};
      } };
#pragma unroll
    for (int ch = 0; ch < NCH; ++ch) prep(ch);
    fin(0, 4);
    SBAR();
    if (w == 2) carry_ld(ts, v);
#pragma unroll
    for (int k = 16; k < 26; ++k) pass1(NCH - 1, k, false);
    SBAR();
    TS(9);
    if (w == 2) carry_in(ts, a32, v);
    wait_bar<63, 0>();
    if (rb == 15 && tid == 0) __hip_atomic_store((gu32*)(a.ws + WS_EPOCH + sg * 256), tag, RLX_AGENT);
#pragma unroll
    for (int n = 0; n < 16; ++n) hread(n);
    fin(4, 5);
  };
  unsigned pfo, pf1 = min(lane, 8) * (SSM ? 128 : 9216);
  { constexpr int LP = 48 / NSH;
    const int ln = min(lane / TG, LP - 1), sub = (lane % TG) * 64, l3 = ln / (LP / 3);
    pfo = sub + (SSM ? (ln % (LP / 3)) * 1152 + (l3 < 2 ? p0 * 2 + l3 * 128 : 1024) : ln * 128); PIN(pfo); }
  char* const flagb = a.ws + WS_FLAG + rb * 4096;
  u32x2 fl;
  auto publish = [&](unsigned n) { flag_store(flagb + slice * 256, n, rtag, w); };
  auto await = [&](u32x2 v, unsigned n0, unsigned n1) {
    const unsigned need = (pvo & 512) ? n1 : n0;
    if (__builtin_expect(!__all(need == 0 || (v[1] == rtag && v[0] >= need)), 0)) {
      for (unsigned spins = 0; spins < (1u << 16); ++spins) {
        const unsigned long long t = __hip_atomic_load((gu64*)(flagb + w * 256 + (pvo & 512) * 4), RLX_AGENT);
        if (__all(need == 0 || ((unsigned)(t >> 32) == rtag && (unsigned)t >= need))) break;
        __builtin_amdgcn_s_sleep(4);
      }
    } };
  const char* const jsrc = a.ws + WS_PART + (size_t)(w * 16 + rb) * PART_BLK + slice * 1024;
  auto jissue = [&](auto j_) { constexpr int j = j_, so = S.jslot[j] * PITCH;
    dma1s(jvo, jsrc + (j >> 1) * 16384 + (size_t)(j & 1) * (128 * PART_BLK), lds0 + w * 1024 + so); };
  sfor<0, C::NM / 4>([&](auto p_) { sfor<4 * p_, 4 * p_ + 4>([&](auto m_) {
    constexpr int m = m_;
    constexpr bool ph1 = m < C::NM1;
    constexpr int un = m / 36, gn = m % 36, m2 = m - C::NM1;
    if constexpr (m == 36) { TS(4); }
    if constexpr (m == C::NM1) { TS(5);
      if constexpr (SSM) { ssm_boundary(); TS(12); }
      int tz = threadIdx.x; PIN(tz);
      pvo = (tz & 63) * 16; jvo = pvo ^ (r << 5) ^ ((tz & 32) << 2); pbase = a.ws + WS_PART + (size_t)(slice * 16 + rb) * PART_BLK + w * 2048; }
    if constexpr (!SSM && m == 180) { PIN(fb2[0]); PIN(fb2[1]); PIN(fb2[2]); PIN(prd); }
    if constexpr (m > 0 && bar_at(m)) {
      constexpr bool lgk0 = (SSM ? m == C::NM1 || m == C::NM1 - 24 : m == 204 || m == 240);
      wait_bar<S.vm[m], (lgk0 ? 0 : 15)>();
      if constexpr (pub_at<SSM>(m)) { constexpr int pc = pub_count<SSM>(m); publish(pc); }
      if constexpr (chk_at<SSM>(m)) { constexpr int n0 = N0[chk_at<SSM>(m)], n1 = N1[chk_at<SSM>(m)]; pin2(fl); await(fl, n0, n1); }
    }
    constexpr bool post = m >= 36 && (ph1 || (!SSM && m2 < 36));
    if constexpr (m + 3 < C::NM) af[(m + 3) & 3] = fread(ic<m + 3>{});
    if constexpr (m + 3 < 36) xread(m + 3);
    if constexpr (SSM && m2 >= 0 && m2 < 4) hread(m2 + 16);
    if constexpr (!SSM) {
      if constexpr (m >= 192 && m < 204) { constexpr int n = m - 192; P[n >> 2][n & 3] = lds128(prd + (pslot(n >> 2) - 11) * PITCH + ((n & 3) >> 1) * PITCH + (n & 1) * 1024); }
      if constexpr (m >= 204 && m < 208) { constexpr int n = m - 204; P[3][n] = lds128(prd + (pslot(3) - 11) * PITCH + (n >> 1) * PITCH + (n & 1) * 1024); }
      if constexpr (m >= 228 && m < 232) { constexpr int n = m - 228; P[4][n] = lds128(prd + (pslot(4) - 11) * PITCH + (n >> 1) * PITCH + (n & 1) * 1024); }
      if constexpr (m >= 240 && m < 244) { constexpr int n = m - 240; P[5][n] = lds128(prd + (pslot(5) - 11) * PITCH + (n >> 1) * PITCH + (n & 1) * 1024); }
      if constexpr (ph1 && un < 5 && gn >= 30 && gn < 34) nbias[gn - 30] = lds128(biasb + (un + 1) * 256 + (gn - 30) * 32);
    }
    if constexpr (ph1) {
      if constexpr (SSM) acc = MFMA(__builtin_bit_cast(bf16x8, xf[gn]), af[m & 3], gn == 0 ? zero16 : acc);
      else {
        if constexpr (gn == 0) {
#pragma unroll
          for (int e = 0; e < 16; ++e) acc[e] = __uint_as_float(nbias[e >> 2][e & 3]); }
        acc = MFMA(af[m & 3], __builtin_bit_cast(bf16x8, xf[gn]), acc); }
    } else {
      constexpr int dp = p2_dp<SSM>(m2 / 4), ch = p2_ch<SSM>(m2 / 4);
      constexpr int bc = SSM ? h_ch(m2 % C::UNIT2) : ch, bk = SSM ? h_kk(m2 % C::UNIT2) : m & 3;
      oacc[dp] = MFMA(af[m & 3], __builtin_bit_cast(bf16x8, P[bc][bk]), (ch == 0 && (m & 3) == 0) ? zero16 : oacc[dp]);
    }
    SBAR();
    if constexpr (m >= 4 * NXP - 12 && m < 24 && (m & 1) == 0) xput(3 + (m >> 2) + 9 * ((m >> 1) & 1));
    if constexpr (ph1 && gn == 35) prev = acc;
    if constexpr (SSM) {
      if constexpr (m == 72) { int tz = threadIdx.x; PIN(tz);
#pragma unroll
        for (int k = 0; k < 4; ++k) *(LAS f32x4*)(lds0 + KSLOT * PITCH + tz * 16 + k * 8192) = (f32x4){u[0][2 * k].r, u[0][2 * k].i, u[0][2 * k + 1].r, u[0][2 * k + 1].i}; }
      if constexpr (post && gn < 35) {
        constexpr int k0 = un < NCH - 1 ? gn * 26 / 35 : gn < 12 ? gn * 16 / 12 : 16 + (gn - 12) * 10 / 23, k1 = un < NCH - 1 ? (gn + 1) * 26 / 35 : gn < 11 ? (gn + 1) * 16 / 12 : 16 + (gn - 11) * 10 / 23;
        sfor<k0, k1>([&](auto k_) { pass1(un - 1, k_, true); }); }
      if constexpr (m == C::NM1 - 24) { int tz = threadIdx.x; PIN(tz); if (w < 2) carry_out(tz, pow32(tz)); } }
    else {
      if constexpr (m >= 72 && m <= 217 && gn < 2) *(LAS u32x4*)(pwr + (pslot(un - 2) - 11) * PITCH + gn * 1024) = pn[gn];
      if constexpr (post && ph1 && gn >= 2 && gn < 34) gelu_ph((gn - 2) >> 2, (gn - 2) & 3);
      if constexpr (post && !ph1 && gn >= 2 && gn < 20) { constexpr int k0 = (gn - 2) * 16 / 9, k1 = (gn - 1) * 16 / 9; sfor<k0, k1>([&](auto k_) { gelu_ph(k_ >> 2, k_ & 3); }); }
      if constexpr (m2 == 20 || m2 == 21) *(LAS u32x4*)(pwr + (pslot(5) - 11) * PITCH + (m2 - 20) * 1024) = pn[m2 - 20];
    }
    if constexpr (store_of<SSM>(m) >= 0) {
      constexpr int dp = store_of<SSM>(m) >> 1, e = (store_of<SSM>(m) & 1) * 8;
      u32x4 o = {pk2(oacc[dp][e], oacc[dp][e + 1]), pk2(oacc[dp][e + 2], oacc[dp][e + 3]), pk2(oacc[dp][e + 4], oacc[dp][e + 5]), pk2(oacc[dp][e + 6], oacc[dp][e + 7])};
      pst(pvo, pbase + dp * 16384 + (e >> 3) * 1024, o);
    }
    if constexpr (pf_of(m) >= 0) {
      const char* sb;
      if constexpr (SSM) { sb = sb2 + ((rb & (NSH - 1)) * 36 + r * 9 + pf_of(m)) * (16 / NSH) * 1152; }
      else sb = sb2 + ((rb & (NSH - 1)) * 72 + w * 9 + pf_of(m)) * (48 / NSH) * 128;
      touch(pfo, sb, lds0 + LDS_BYTES - 256);
    }
    if constexpr (pf1_of(m) >= 0) {
      constexpr int k = pf1_of(m);
      if constexpr (SSM) { if constexpr (k < 4) touch(pf1, (const char*)(c ? a.bim : a.bre) + (size_t)((k < 3 ? p0 + 32 * (k + 1) : p4) + (rb & 7) * 4 + r) * 1152, lds0 + LDS_BYTES - 256); else touch(pf1, sb1, lds0 + LDS_BYTES - 256); }
      else touch(pf1, sb1 + (k + 1) * 128 + (size_t)((rb & 7) * 72 + w * 9) * 9216, lds0 + LDS_BYTES - 256);
    }
    if constexpr (fld_at<SSM>(m)) gload8s(fl, (pvo & 512) * 4, flagb + w * 256);
    if constexpr (job_at<SSM>(m) >= 0) jissue(ic<JORD[job_at<SSM>(m) < 0 ? 0 : job_at<SSM>(m)]>{});
    sfor<S.fis[m], S.fis[m] + S.nis[m]>(issue);
    SBAR();
  }); });
  TS(13); TS(6);
#pragma unroll
  for (int hf = 0; hf < 2; ++hf) { const int e = hf * 8;
    u32x4 o = {pk2(oacc[8][e], oacc[8][e + 1]), pk2(oacc[8][e + 2], oacc[8][e + 3]), pk2(oacc[8][e + 4], oacc[8][e + 5]), pk2(oacc[8][e + 6], oacc[8][e + 7])};
    pst(pvo, pbase + 8 * 16384 + hf * 1024, o); }
  gload8s(fl, (pvo & 512) * 4, flagb + w * 256);
  float* const outb = kargs()->out + (size_t)t0 * D;
  tid = threadIdx.x; PIN(tid); lane = tid & 63;
  unsigned eo = ((jt0 + (tid >> 4)) * D + jd0 + (tid & 15)) * 2, eo4 = eo * 2; PIN(eo); PIN(eo4);
  const f32x2 stt = *(LAS f32x2*)(lds0 + FST + (tid >> 4) * 8);
  float xv[9], eb[9], ed[9]; unsigned ja[9], je[9];
  u32x4 sel[2];
#pragma unroll
  for (int b = 0; b < 2; ++b) { const float o = __uint_as_float(0x3f803f80u);
    const unsigned lo = __float_as_uint(selm(0.f, o, 0x100000001ull << (4 * b))), hi = __float_as_uint(selm(0.f, o, 0x200000001ull << (4 * b))); sel[b] = (u32x4){lo, lo, hi, hi}; PIN(sel[b]); }
  const unsigned jb = ((lane & 15) >> 2) * 1024 + (((4 * w + ((lane >> 4) & 1) + 32 * (lane & 1)) ^ ((lane & 12) >> 1) ^ ((lane & 1) << 3)) << 4) + ((lane >> 1) & 1) * 8;
  sfor<0, 9>([&](auto dp_) { constexpr int dp = dp_, s0 = S.jslot[2 * dp] * PITCH, s1 = S.jslot[2 * dp + 1] * PITCH;
    const unsigned so = lds0 + s0 + (lane >> 5) * (s1 - s0); ja[dp] = so + jb; je[dp] = so + (jb ^ 32); pin1(ja[dp]); pin1(je[dp]);
    const unsigned e = *(LAS unsigned*)(lds0 + ETAB + (dp * 16 + (lane & 15)) * 4); eb[dp] = lo2f(e); ed[dp] = hi2f(e); pinf(eb[dp]); pinf(ed[dp]); xv[dp] = 0.f; pinf(xv[dp]); });
  wait_bar<0, 0>();
  publish(9);
  TS(7);
  pin2(fl); await(fl, MAXPUB, MAXPUB);
  sfor<0, 9>([&](auto dp_) { gload_hi<dp_ * 128>(xv[dp_], eo, xb); });
  sfor<NJIN, 2 * MAXPUB>([&](auto k_) { jissue(ic<JORD[k_]>{}); });
  asm volatile("s_waitcnt vmcnt(%0)" :: "n"(2 * MAXPUB - NJIN) : "memory");
#pragma unroll
  for (int dp = 0; dp < 9; ++dp) { PIN(xv[dp]); ed[dp] = rbf(rbf(rbf(xv[dp] - stt[0]) * stt[1]) * ed[dp]); PIN(ed[dp]); }
  auto finish = [&](auto d0_, auto d1_) { constexpr int d0 = d0_, d1 = d1_;
    bf16x8 b0[9], b1[9]; f32x16 t[9]; float uu[9], ee[9];
#pragma unroll
    for (int dp = d0; dp < d1; ++dp) { b0[dp] = cat(trd(ja[dp]), trd(ja[dp] + 4096)); b1[dp] = cat(trd(je[dp]), trd(je[dp] + 4096)); }
    SBAR();
#pragma unroll
    for (int dp = d0; dp < d1; ++dp) t[dp] = MFMA(__builtin_bit_cast(bf16x8, sel[0]), b0[dp], zero16);
#pragma unroll
    for (int dp = d0; dp < d1; ++dp) t[dp] = MFMA(__builtin_bit_cast(bf16x8, sel[1]), b1[dp], t[dp]);
    SBAR();
#pragma unroll
    for (int dp = d0; dp < d1; ++dp) uu[dp] = rbf(rbf(t[dp][0] * IBETA) + eb[dp]);
#pragma unroll
    for (int dp = d0; dp < d1; ++dp) ee[dp] = __builtin_amdgcn_exp2f(uu[dp] * -1.4426950409f);
#pragma unroll
    for (int dp = d0; dp < d1; ++dp) ee[dp] = __builtin_amdgcn_rcpf(1.f + ee[dp]);
    sfor<d0, d1>([&](auto dp_) { constexpr int dp = dp_; gstore4<dp * 256>(eo4, outb, xv[dp] + (t[dp][1] + ed[dp]) * rbf(uu[dp] * ee[dp])); });
    SBAR(); };
  constexpr int F0 = NJIN >= 8 ? NJIN / 2 : NJIN >= 7 ? 3 : 0;
  finish(ic<0>{}, ic<F0>{});
  await((u32x2){0u, 0u}, 9, 9);
  TS(10);
  sfor<2 * MAXPUB, 18>([&](auto k_) { jissue(ic<JORD[k_]>{}); });
  wait_bar<18 - 2 * MAXPUB, 15>();
  flag_store(a.ws + WS_REPOCH + rb * 256, rtag, rtag, w | slice);
  finish(ic<F0>{}, ic<MAXPUB>{});
  wait_bar<MAXPUB - F0, 15>();
  finish(ic<MAXPUB>{}, ic<9>{});
  TS(11);
}
#undef S
__global__ __launch_bounds__(512, 2) void k_main(Args a) {
  __shared__ __attribute__((aligned(16))) char smem[LDS_BYTES];
  const unsigned lds0 = (unsigned)(uintptr_t)smem;
  const int bid = blockIdx.x, xcd = bid & 7, loc = bid >> 3;
  const int slice = (loc & 3) * 4 + (xcd & 3), rb = (xcd >> 2) * 8 + (loc >> 2);
  if (slice < NFFN) body<false>(lds0, slice, rb); else body<true>(lds0, slice, rb);
}

extern "C" void kernel_launch(void* const* d_in, const int* in_sizes, int n_in, void* d_out, int out_size, void* d_ws, size_t ws_size, hipStream_t stream) {
  Args a;
  a.x = (const u16*)d_in[0]; a.lre = (const u16*)d_in[1]; a.lim = (const u16*)d_in[2]; a.bre = (const u16*)d_in[3]; a.bim = (const u16*)d_in[4];
  a.cre = (const u16*)d_in[5]; a.cim = (const u16*)d_in[6]; a.ldt = (const u16*)d_in[7]; a.dsk = (const u16*)d_in[8];
  a.w1 = (const u16*)d_in[10]; a.b1 = (const u16*)d_in[11]; a.w2 = (const u16*)d_in[12]; a.b2 = (const u16*)d_in[13];
  a.ws = (char*)d_ws; a.out = (float*)d_out;
  k_main<<<256, 512, 0, stream>>>(a);
}
#define NSTAMP 16
#define STAMP_VALID(b) 1
#define STAMP_CLASS(b) ((((b) >> 3) & 3) == 3)
```

```cpp
#define SC1ST
#define CODEPF 61440
#define OSC1
#define STOUCH
#define FINE 24
#define PF2S 74
#define PF2D 8
#define MX12 5
#define PARK 1
#define PFD 3
#define CT1 1000
#define CT2 1000
#define NSH 16
#define TG 1
#define ROT 4
#define PF1S 0
#include <hip/hip_runtime.h>
#include <stdint.h>
#include <type_traits>

typedef unsigned short u16;
typedef short bf16x8 __attribute__((ext_vector_type(8)));
typedef short s16x4 __attribute__((ext_vector_type(4)));
typedef float f32x16 __attribute__((ext_vector_type(16)));
typedef float f32x4 __attribute__((ext_vector_type(4)));
typedef float f32x2 __attribute__((ext_vector_type(2)));
typedef unsigned u32x4 __attribute__((ext_vector_type(4)));
typedef unsigned u32x2 __attribute__((ext_vector_type(2)));
typedef __bf16 bf2 __attribute__((ext_vector_type(2)));
#define LAS __attribute__((address_space(3)))
#define GAS __attribute__((address_space(1)))
#define DI __device__ __forceinline__
typedef GAS unsigned gu32;
typedef GAS unsigned long long gu64;
#define RLX_AGENT __ATOMIC_RELAXED, __HIP_MEMORY_SCOPE_AGENT
template <int V> using ic = std::integral_constant<int, V>;

constexpr int L = 2048, D = 576, F = 4608;
constexpr int NFFN = 12, NSL = 16, FS = 384, NCH = 5, NSIG = 32 * NCH;
constexpr int LDS_BYTES = 163840;
constexpr size_t PART_BLK = 9 * 8 * 2048;
constexpr size_t WS_PART = 0;
constexpr size_t WS_STAT = WS_PART + (size_t)NSL * 16 * PART_BLK;
constexpr size_t WS_EPOCH = WS_STAT + L * 8;
constexpr size_t WS_GRAN = WS_EPOCH + 1024;
constexpr size_t WS_REPOCH = WS_GRAN + 4 * 16 * 2 * NSIG * 8;
constexpr size_t WS_FLAG = WS_REPOCH + 16 * 256;
constexpr size_t WS_DBG = WS_FLAG + 16 * 16 * 256;
constexpr float BETA = 0.46866869f;
constexpr float KAP = 4.9122295f;
constexpr float IBETA = 1.0f / BETA;

#define SBAR() __builtin_amdgcn_sched_barrier(0)
#define PIN(x) asm volatile("" : "+v"(x))
#define MFMA(a, b, c) __builtin_amdgcn_mfma_f32_32x32x16_bf16(a, b, c, 0, 0, 0)

DI float lo2f(unsigned v) { return __uint_as_float(v << 16); }
DI float hi2f(unsigned v) { return __uint_as_float(v & 0xffff0000u); }
DI unsigned pk2(float a, float b) { bf2 r = {(__bf16)a, (__bf16)b}; return __builtin_bit_cast(unsigned, r); }
DI float rbf(float a) { return (float)(__bf16)a; }
DI void dma1(unsigned voff, const char* sb, unsigned dst) {
  asm volatile("s_mov_b32 m0, %2\n\ts_nop 0\n\tglobal_load_lds_dwordx4 %0, %1" :: "v"(voff), "s"(sb), "s"(dst) : "memory");
}
DI void dma1v(const char* src, unsigned dst) {
  asm volatile("s_mov_b32 m0, %1\n\ts_nop 0\n\tglobal_load_lds_dwordx4 %0, off" :: "v"(src), "s"(dst) : "memory");
}
template <int I, int N, class Fn> DI void sfor(Fn&& f) { if constexpr (I < N) { f(ic<I>{}); sfor<I + 1, N>(f); } }
template <int V, int G> DI void wait_bar() { SBAR(); asm volatile("s_waitcnt vmcnt(%0) lgkmcnt(%1)\n\ts_barrier" :: "n"(V), "n"(G) : "memory"); SBAR(); }
DI s16x4 trd(unsigned a) { return __builtin_amdgcn_ds_read_tr16_b64_v4i16((LAS s16x4*)a); }
DI bf16x8 cat(s16x4 lo, s16x4 hi) { return __builtin_shufflevector(lo, hi, 0, 1, 2, 3, 4, 5, 6, 7); }
DI u32x4 lds128(unsigned a) { return *(LAS u32x4*)a; }
DI void pst(unsigned voff, char* sb, u32x4 v) { asm volatile("global_store_dwordx4 %0, %1, %2 sc1\n\ts_nop 1" :: "v"(voff), "v"(v), "s"(sb) : "memory"); }
template <int OFF> DI void gload16(u32x4& d, unsigned voff, const char* sb) { asm volatile("global_load_dwordx4 %0, %1, %2 offset:%3" : "=v"(d) : "v"(voff), "s"(sb), "n"(OFF) : "memory"); }
template <int N> DI void wait6(u32x4* v) { asm volatile("s_waitcnt vmcnt(%6)" : "+v"(v[0]), "+v"(v[1]), "+v"(v[2]), "+v"(v[3]), "+v"(v[4]), "+v"(v[5]) : "n"(N) : "memory"); }
template <int N> DI void wait3(u32x4* v) { asm volatile("s_waitcnt vmcnt(%3)" : "+v"(v[0]), "+v"(v[1]), "+v"(v[2]) : "n"(N) : "memory"); }
DI void pin4(f32x2* t) { asm volatile("" : "+v"(t[0]), "+v"(t[1]), "+v"(t[2]), "+v"(t[3])); }
DI f32x4 mf4(u32x2 x, u32x2 y, f32x4 z) { return __builtin_amdgcn_mfma_f32_4x4x4bf16_1k(__builtin_bit_cast(s16x4, x), __builtin_bit_cast(s16x4, y), z, 0, 0, 0); }
DI void pin1(unsigned& v) { PIN(v); }
DI void stamp2(unsigned ad) { unsigned long long t; asm volatile("s_memtime %0\n\ts_waitcnt lgkmcnt(0)" : "=s"(t) :: "memory"); *(LAS unsigned*)ad = (unsigned)t; }
DI void dma1s(unsigned voff, const char* sb, unsigned dst) {
  asm volatile("s_mov_b32 m0, %2\n\ts_nop 0\n\tglobal_load_lds_dwordx4 %0, %1 sc1" :: "v"(voff), "s"(sb), "s"(dst) : "memory");
}
DI void gload8s(u32x2& d, unsigned voff, const char* sb) { asm volatile("global_load_dwordx2 %0, %1, %2 sc1" : "=v"(d) : "v"(voff), "s"(sb) : "memory"); }
DI void pin2(u32x2& v) { PIN(v); }
DI void flag_store(char* p, unsigned n, unsigned tag, int w) {
  u32x2 v = {n, tag}; unsigned z = 0; unsigned long long keep;
  asm volatile("s_cmp_lg_u32 %4, 0\n\ts_cbranch_scc1 1f\n\ts_mov_b64 %0, exec\n\ts_mov_b64 exec, 1\n\tglobal_store_dwordx2 %1, %2, %3 sc1\n\ts_mov_b64 exec, %0\n1:"
               : "=&s"(keep) : "v"(z), "v"(v), "s"(p), "s"(w) : "memory", "scc");
}
constexpr unsigned long long GMASK = 0xFFFF0000FFFF0000ull, HMASK = 0xFFFFFFFF00000000ull;
DI float selm(float a, float b, unsigned long long m) { float r; asm("v_cndmask_b32 %0, %1, %2, %3" : "=v"(r) : "v"(a), "v"(b), "s"(m)); return r; }
DI void touch(unsigned voff, const char* sb, unsigned dst) { asm volatile("s_mov_b32 m0, %2\n\ts_nop 0\n\tglobal_load_lds_dword %0, %1" :: "v"(voff), "s"(sb), "s"(dst) : "memory"); }
template <int OFF> DI void gload_hi(float& d, unsigned voff, const char* sb) { asm volatile("global_load_short_d16_hi %0, %1, %2 offset:%3" : "+v"(d) : "v"(voff), "s"(sb), "n"(OFF) : "memory"); }
#define OFL " sc1"
template <int OFF> DI void gstore4(unsigned voff, float* sb, float v) { asm volatile("global_store_dword %0, %1, %2 offset:%3" OFL :: "v"(voff), "v"(v), "s"(sb), "n"(OFF) : "memory"); }
DI void pinf(float& v) { PIN(v); }
template <int CTRL> DI float dpp(float v) { return __int_as_float(__builtin_amdgcn_update_dpp(0, __float_as_int(v), CTRL, 0xf, 0xf, true)); }
struct cf { float r, i; };
DI cf cmul(cf a, cf b) { return {__builtin_fmaf(a.r, b.r, -a.i * b.i), __builtin_fmaf(a.r, b.i, a.i * b.r)}; }
DI cf csq(cf a) { return {__builtin_fmaf(a.r, a.r, -a.i * a.i), (a.r + a.r) * a.i}; }
DI cf cmad(cf a, cf b, cf c) { return {__builtin_fmaf(a.r, b.r, __builtin_fmaf(-a.i, b.i, c.r)), __builtin_fmaf(a.r, b.i, __builtin_fmaf(a.i, b.r, c.i))}; }
DI void swap32(float x, float& lo, float& hi) { auto r = __builtin_amdgcn_permlane32_swap(__float_as_uint(x), __float_as_uint(x), false, false); lo = __uint_as_float(r[0]); hi = __uint_as_float(r[1]); }
DI void swap16(float x, float y, float& a, float& b) { auto r = __builtin_amdgcn_permlane16_swap(__float_as_uint(x), __float_as_uint(y), false, false); a = __uint_as_float(r[0]); b = __uint_as_float(r[1]); }

template <bool SSM> struct Cfg {
  static constexpr int NP = SSM ? 18 * NCH : 108, NP1 = NP / 2, NM = 4 * NP, NM1 = NM / 2, NI = NP;
  static constexpr int PITCH = SSM ? 8448 : 8192, NSLOT = SSM ? 18 : 19;
  static constexpr int UNIT2 = SSM ? 4 * NCH : 24;
  static constexpr int MISC = NSLOT * PITCH;
};
constexpr bool bar_at(int m) { return m % 12 == 0 || (m < FINE && m % 4 == 0); }
constexpr int next_bar(int m) { int b = m + 1; while (!bar_at(b)) ++b; return b; }
constexpr int item_piece(int q) { return q; }
constexpr int witem(int p) { return p; }
constexpr int pslot(int ch) { return ch == 0 ? 17 : ch == 1 ? 15 : ch == 2 ? 13 : ch == 3 ? 11 : ch == 4 ? 17 : 15; }
constexpr int HSLOT = 8, KSLOT = 14;
template <bool SSM> constexpr int p2_dp(int j) { return SSM ? j / NCH : (j < 12 ? (j & 1) : j / 6); }
template <bool SSM> constexpr int p2_ch(int j) { return SSM ? j % NCH : (j < 12 ? (j >> 1) : j % 6); }
constexpr int h_ch(int n) { return n >= 16 ? 4 : 2 * (n >> 3) + ((n >> 1) & 1); }
constexpr int h_kk(int n) { return n >= 16 ? n & 3 : 2 * ((n >> 2) & 1) + (n & 1); }
template <bool SSM> constexpr int store_of(int m) {
  using C = Cfg<SSM>; const int m2 = m - C::NM1;
  if (m2 < 0) return -1;
  if (!SSM && m2 < 72) return m2 == 50 ? 0 : m2 == 54 ? 1 : m2 == 58 ? 2 : m2 == 62 ? 3 : -1;
  if (m2 < C::UNIT2) return -1;
  return m2 % C::UNIT2 == 4 ? 2 * (m2 / C::UNIT2 - 1) : m2 % C::UNIT2 == 8 ? 2 * (m2 / C::UNIT2 - 1) + 1 : -1;
}
#define NPF 9
constexpr int pf_of(int m) { return (m >= PF2S && m < PF2S + PF2D * NPF && (m - PF2S) % PF2D == 0) ? (m - PF2S) / PF2D : -1; }
#define NPF1 0
constexpr int pf1_of(int m) { return (m >= PF1S && m < PF1S + 4 * NPF1 && m % 4 == 0) ? (m - PF1S) / 4 : -1; }
#define NXP 6
#define MX4 (1 + ((B >> 2) & 1))
#define I0 (C::NSLOT - 2 * NXP)
constexpr int LOOK = 18;
constexpr int xslot(int p) { return p < NXP ? 2 * p : 2 * p - 8; }
constexpr int JORD[18] = {0, 2, 4, 6, 1, 3, 5, 7, 8, 9, 10, 11, 12, 13, 14, 15, 16, 17};
#define JV 0
constexpr int JC[10] = {0, 0, 0, 2, 2, 3, 0, 0, 0, 0}, N0[10] = {0, 0, 0, 0, 0, 4, 0, 0, 0, 0}, N1[10] = {0, 0, 0, 0, 0, 3, 0, 0, 0, 0}, FD[10] = {0, 0, 0, 0, 0, 10, 0, 0, 0, 0};
constexpr int JE0 = 5, MAXPUB = 6;
constexpr int jcount(int e) { return e >= 0 && e < 10 ? JC[e] : 0; }
constexpr int jfirst(int e) { int n = 0; for (int k = 9; k > e; --k) n += JC[k]; return n; }
constexpr int NJIN = jfirst(0);
template <bool SSM> constexpr int last_store(int dp) { for (int m = Cfg<SSM>::NM - 1; m >= 0; --m) if (store_of<SSM>(m) == 2 * dp + 1) return m; return 100000; }
template <bool SSM> constexpr int pub_count(int B) { int n = 0;
  while (n < MAXPUB && last_store<SSM>(n) + (last_store<SSM>(n) < Cfg<SSM>::NM - 12 * JE0 ? 14 : SSM ? 20 : 26) <= B) ++n; return n; }
template <bool SSM> constexpr bool pub_at(int B) { return B % 12 == 0 && (pub_count<SSM>(B) == (SSM ? 3 : 4) || pub_count<SSM>(B) == MAXPUB) && pub_count<SSM>(B) != pub_count<SSM>(B - 12); }
template <bool SSM> constexpr int chk_at(int B) { const int e = (Cfg<SSM>::NM - B) / 12; return (B % 12 == 0 && e > 0 && e < 10 && FD[e]) ? e : 0; }
template <bool SSM> constexpr int fld_of(int B) { return B - FD[chk_at<SSM>(B)]; }
template <bool SSM> constexpr bool fld_at(int m) { for (int e = 1; e < 10; ++e) if (FD[e] && Cfg<SSM>::NM - 12 * e - FD[e] == m) return true; return false; }
template <bool SSM> constexpr int job_at(int m) {
  const int e = (Cfg<SSM>::NM - (m - m % 12)) / 12, k = (m % 12) / 4; return (m % 4 == 2 && k < jcount(e)) ? jfirst(e) + k : -1; }
template <bool SSM> constexpr int xld_at(int m) { return (m == Cfg<SSM>::NM - CT1 || m == Cfg<SSM>::NM - CT2) ? 0 : -1; }
struct Sched { int slot[126]; int istep[126]; int vm[432]; int nis[432]; int fis[432]; int jslot[18]; bool ok; };
template <bool SSM> constexpr Sched make_sched() {
  using C = Cfg<SSM>;
  Sched s{};
  int free_at[19] = {}, b0[19] = {}, b1[19] = {};
  for (int i = 0; i < 19; ++i) { free_at[i] = i < 2 * NXP ? ((i >= 2 * NXP - 8 && i < 10) ? 20 + 4 * (i >> 1) : 4 + 4 * (i >> 1)) : -1; b0[i] = 1000; b1[i] = 1000; }
  if (SSM) { for (int i = HSLOT; i < 18; ++i) { b0[i] = (PARK && i >= KSLOT) ? 72 : C::NM1; b1[i] = C::NM1 + (i < HSLOT + 7 ? 0 : 12); } }
  else { b0[17] = b0[18] = 72; b1[17] = b1[18] = 240; b0[15] = b0[16] = 108; b1[15] = b1[16] = 264; b0[13] = b0[14] = 144; b1[13] = b1[14] = 204; b0[11] = b0[12] = 180; b1[11] = b1[12] = 216; }
  int q = 0; bool jok = true;
  for (int B = -1; B < C::NM; B = B < 0 ? 0 : next_bar(B)) {
    const int len = B < 0 ? I0 : next_bar(B) - B, maxi = B < 0 ? I0 : len == 4 ? MX4 : MX12;
    if (B >= 36) for (int k = 0; k < jcount((C::NM - B) / 12); ++k) {
      int best = -1; for (int i = 0; i < C::NSLOT; ++i) if (free_at[i] <= B) { best = i; break; }
      if (best < 0) jok = false; else { s.jslot[JORD[jfirst((C::NM - B) / 12) + k]] = best; free_at[best] = 100000; } }
    int n = 0;
    while (n < maxi && q + n < C::NI && item_piece(q + n) < (B < 0 ? 0 : B) / 4 + 18) {
      const int it = q + n, end = next_bar(4 * item_piece(it) + 3); int best = -1;
      int key = 100000;
      for (int i = C::NSLOT - 1; i >= 0; --i) if (free_at[i] <= B && (end <= b0[i] || B >= b1[i]) && !(SSM && it >= C::NP1 - (PARK ? 14 : 17) && it < C::NP1 && (i >= HSLOT) != (it >= C::NP1 - (PARK ? 6 : 9)))) { const int k2 = B >= b1[i] ? 1000 : b0[i]; if (k2 < key) { key = k2; best = i; } }
      if (best < 0) break;
      s.slot[it] = best; free_at[best] = end; ++n;
    }
    for (int k = 0; k < n; ++k) s.istep[q + k] = B < 0 ? -1 : len == 4 ? B + 2 * k + 1 : B + (n <= 3 ? 4 * k + 3 : n == 4 ? 3 * k + 1 : 2 * k + 1);
    q += n;
  }
  for (int k = NJIN; k < 18; ++k) { int best = -1; for (int i = 0; i < C::NSLOT; ++i) if (free_at[i] <= C::NM) { best = i; break; }
    if (best < 0) jok = false; else { s.jslot[JORD[k]] = best; free_at[best] = 100000; } }
  s.ok = q == C::NI && jok;
  for (int m = 0; m < C::NM; ++m) { s.nis[m] = 0; s.fis[m] = 0; }
  for (int it = C::NI - 1; it >= 0; --it) if (s.istep[it] >= 0) { s.nis[s.istep[it]]++; s.fis[s.istep[it]] = it; }
  for (int B = 0; B < C::NM; B = next_bar(B)) {
    int np = next_bar(B) / 4; if (np > C::NP - 1) np = C::NP - 1;
    const int need = witem(np);
    if (s.istep[need] >= B) { s.ok = false; continue; }
    int cnt = 0;
    for (int it = need + 1; it < C::NI; ++it) if (s.istep[it] < B) ++cnt;
    for (int m = s.istep[need] + 1; m < B; ++m) if (m >= 0) cnt += (store_of<SSM>(m) >= 0) + (pf_of(m) >= 0) + (pf1_of(m) >= 0) + fld_at<SSM>(m) + (job_at<SSM>(m) >= 0) + (xld_at<SSM>(m) >= 0);
    for (int t = 0; t < 2; ++t) {
      const int m0 = t == 0 ? (pub_at<SSM>(B) ? last_store<SSM>(pub_count<SSM>(B) - 1) : -1) : (chk_at<SSM>(B) ? fld_of<SSM>(B) : -1);
      if (m0 < 0) continue;
      int c2 = (t == 0 ? fld_at<SSM>(m0) : 0) + (job_at<SSM>(m0) >= 0) + s.nis[m0];
      for (int m = m0 + 1; m < B; ++m) c2 += (store_of<SSM>(m) >= 0) + fld_at<SSM>(m) + (job_at<SSM>(m) >= 0) + (xld_at<SSM>(m) >= 0) + s.nis[m];
      if (c2 < cnt) cnt = c2;
    }
    s.vm[B] = cnt; if (cnt > 63) s.ok = false;
  }
  return s;
}
template <bool SSM> struct SC { static constexpr Sched s = make_sched<SSM>(); };
static_assert(SC<false>::s.ok && SC<true>::s.ok, "bad schedule");

#define TS(i)

struct Args {
  const u16 *x, *lre, *lim, *bre, *bim, *cre, *cim, *ldt, *dsk, *w1, *b1, *w2, *b2;
  char* ws; float* out;
};

typedef const __attribute__((address_space(4))) Args* KArgs;
DI KArgs kargs() { KArgs p = (KArgs)__builtin_amdgcn_kernarg_segment_ptr(); asm volatile("" : "+s"(p)); return p; }
template <bool SSM> DI void body(const unsigned lds0, const int slice, const int rb, const char* code) {
  using C = Cfg<SSM>;
#define S SC<SSM>::s
  Args a;
  { KArgs k = kargs(); a.x = k->x; a.ws = k->ws;
    if constexpr (SSM) { a.lre = k->lre; a.lim = k->lim; a.ldt = k->ldt; a.bre = k->bre; a.bim = k->bim; a.cre = k->cre; a.cim = k->cim; }
    else { a.w1 = k->w1; a.b1 = k->b1; a.w2 = k->w2; }
    a.b2 = k->b2; a.dsk = k->dsk;
    if constexpr (SSM) asm volatile("" : "+s"(a.lre), "+s"(a.lim), "+s"(a.ldt), "+s"(a.b2), "+s"(a.dsk)); else asm volatile("" : "+s"(a.b1), "+s"(a.b2), "+s"(a.dsk)); }
  constexpr int PITCH = C::PITCH, TAB = C::MISC, TAB1 = TAB + NSIG * 16, SEG = TAB1 + NSIG * 16, FST = LDS_BYTES - 512, ETAB = FST - 576, CSX = ETAB - 320;
  static_assert(!SSM || SEG + 4 * NSIG * 8 <= CSX, "");
  int tid = threadIdx.x, lane = tid & 63;
  const int w = __builtin_amdgcn_readfirstlane(tid >> 6), r = w & 3, c = w >> 2;
  TS(0);
  const int f0 = slice * FS, t0 = rb * 128;
  const int sg = slice - NFFN, p0 = 128 * sg, p4 = 512 + 32 * (sg & 1);
  int h = lane >> 5, l31 = lane & 31;
  const int drow = 8 * w + (lane >> 3), dcp = lane & 7;
  const unsigned voffX = (lane >> 3) * 1152 + ((dcp ^ ((drow >> 1) & 7)) << 4);
  const int xrot = ROT * (slice >> 2);
  const int xblk[2] = {(w + xrot) & 15, (w + 8 + xrot) & 15};
  const char* xb = (const char*)(a.x + (size_t)t0 * D);
  const unsigned ddst = lds0 + w * (SSM ? 1056 : 1024);
  unsigned voff1, voff2; const char *sb1, *sb2;
  if constexpr (SSM) {
    const int sw = dcp ^ ((drow >> 1) & 1);
    voff1 = (16 * c + 8 * (w & 1) + (lane >> 3)) * 1152 + (sw << 4);
    sb1 = (const char*)(((w >> 1) & 1) ? a.bim : a.bre);
    sb2 = (const char*)(c ? a.cim : a.cre);
  } else {
    const unsigned sw = (dcp ^ (((drow >> 1) & 1) << 2)) << 4;
    voff1 = drow * 9216 + sw; voff2 = drow * 1152 + sw;
    sb1 = (const char*)(a.w1 + f0); sb2 = (const char*)(a.w2 + (size_t)f0 * D);
  }
  auto issue = [&](auto it_) {
    constexpr int p = it_, q = p - C::NP1, so = S.slot[p] * PITCH;
    if constexpr (SSM) {
      if constexpr (p < C::NP1) dma1(voff1, sb1 + (p / 9 < 4 ? p0 + (p / 9) * 32 : p4) * 1152 + (p % 9) * 128, ddst + so);
      else {
        if constexpr (q == 0) { int tz = threadIdx.x; PIN(tz); const int dr = (tz >> 2) & 63, d8 = tz >> 3;
          voff2 = dr * 1152 + (((tz & 3) ^ ((dr >> 3) & 1)) << 4); voff1 = d8 * 1152 + (((tz & 7) ^ ((d8 >> 1) & 1)) << 4); }
        constexpr int dp = q / NCH, j = q % NCH;
        if constexpr (j < 4) dma1(voff1, (const char*)((j & 1) ? a.cim : a.cre) + dp * (64 * 1152) + p0 * 2 + (j >> 1) * 128, ddst + so);
        else dma1(voff2, sb2 + dp * (64 * 1152) + p4 * 2, ddst + so);
      }
    } else {
      if constexpr (p < C::NP1) dma1(voff1, sb1 + (p / 9) * 128 + (size_t)(p % 9) * (64 * 9216), ddst + so);
      else dma1(voff2, sb2 + p2_dp<SSM>(q) * 128 + p2_ch<SSM>(q) * (64 * 1152), ddst + so);
    }
  };
  const int jd0 = (slice >> 3) * 32 + (slice & 1) * 16, jt0 = ((slice >> 1) & 3) * 32;
  unsigned rtag = *(const __attribute__((address_space(4))) unsigned*)(a.ws + WS_REPOCH + rb * 256) + 1u;
  unsigned tag = 0;
  if constexpr (SSM) tag = *(const __attribute__((address_space(4))) unsigned*)(a.ws + WS_EPOCH + sg * 256) + 1u;
  u32x2 braw; unsigned plr, pli, pdt, eb2, edk;
  if (w < 3) {
    if constexpr (SSM) {
      const int sp = tid < 128 ? p0 + tid : p4 + (tid & 31);
      asm volatile("global_load_ushort %0, %1, off" : "=v"(plr) : "v"(a.lre + sp) : "memory");
      asm volatile("global_load_ushort %0, %1, off" : "=v"(pli) : "v"(a.lim + sp) : "memory");
      asm volatile("global_load_ushort %0, %1, off" : "=v"(pdt) : "v"(a.ldt + sp) : "memory");
    } else {
      asm volatile("global_load_dwordx2 %0, %1, off" : "=v"(braw) : "v"(a.b1 + f0 + 4 * (tid < 96 ? tid : 95)) : "memory");
    }
    const int ti = min(tid, 143), d = (ti >> 4) * 64 + jd0 + (ti & 15);
    asm volatile("global_load_ushort %0, %1, off" : "=v"(eb2) : "v"(a.b2 + d) : "memory");
    asm volatile("global_load_ushort %0, %1, off" : "=v"(edk) : "v"(a.dsk + d) : "memory");
  } else if (w == 3) {
    touch(min(lane, CODEPF / 4096 - 1) * 128, code, lds0 + LDS_BYTES - 256);
  }
  u32x4 xr[18];
  sfor<0, 9>([&](auto k_) { gload16<k_ * 128>(xr[k_], voffX, xb + xblk[0] * 9216); });
  u32x4 xf[36];
  unsigned xa[2][4];
  {
    const int xrow = 32 * (r & 1) + (SSM ? 16 * ((l31 >> 2) & 1) + (l31 & 3) + 4 * (l31 >> 3) : l31), xkey = (xrow >> 1) & 7;
#pragma unroll
    for (int kk = 0; kk < 4; ++kk) { xa[0][kk] = lds0 + (r >> 1) * PITCH + xrow * 128 + (((2 * kk + h) ^ xkey) << 4); xa[1][kk] = xa[0][kk] + 8 * PITCH; PIN(xa[0][kk]); PIN(xa[1][kk]); } }
  unsigned fb1[3], fb2[3];
  if constexpr (SSM) {
    const int row = 32 * c + l31;
#pragma unroll
    for (int i = 0; i < 3; ++i) fb1[i] = lds0 + (row >> 3) * 1056 + (row & 7) * 128 + ((h ^ ((row >> 1) & 1)) << 4) + i * 65536;
  } else {
    const int q4 = (lane & 15) >> 2, pq = lane & 3, cb = (lane >> 4) & 1;
    const unsigned colb = (((c * 4 + cb * 2 + (pq >> 1)) ^ ((q4 >> 1) << 2)) << 4) + 8 * (pq & 1);
#pragma unroll
    for (int i = 0; i < 3; ++i) { fb1[i] = lds0 + (8 * h + q4) * 128 + colb + i * 65536; fb2[i] = lds0 + (4 * h + q4) * 128 + colb + i * 65536; }
  }
  PIN(fb1[0]); PIN(fb1[1]); PIN(fb1[2]); PIN(voff1);
  auto fread = [&](auto m_) -> bf16x8 {
    constexpr int p = m_ / 4, kk = m_ & 3;
    constexpr bool mixed = SSM && p >= C::NP1 && (p - C::NP1) % NCH == 4;
    constexpr int off = S.slot[p] * PITCH + (!SSM ? kk * 2048 : !mixed ? kk * 32 : (kk >> 1) * 4224 + (kk & 1) * 32);
    if constexpr (SSM && m_ == C::NM1 + 16) { int tz = threadIdx.x; PIN(tz); const int row = 32 * c + (tz & 31);
#pragma unroll
      for (int i = 0; i < 3; ++i) { fb2[i] = lds0 + (row >> 4) * 1056 + (row & 15) * 64 + ((((tz >> 5) ^ (row >> 3)) & 1) << 4) + i * 65536; pin1(fb2[i]); } }
    if constexpr (mixed) return __builtin_bit_cast(bf16x8, lds128(fb2[off >> 16] + (off & 65535)));
    else if constexpr (SSM) return __builtin_bit_cast(bf16x8, lds128(fb1[off >> 16] + (off & 65535)));
    else if constexpr (p < C::NP1) return cat(trd(fb1[off >> 16] + (off & 65535)), trd(fb1[off >> 16] + (off & 65535) + 512));
    else return cat(trd(fb2[off >> 16] + (off & 65535)), trd(fb2[off >> 16] + (off & 65535) + 1024)); };
  auto xread = [&](int m) { const int sl = xslot(m >> 2); xf[m] = lds128(xa[sl >> 3][m & 3] + (sl & 7) * PITCH); };
  unsigned pwr, prd, biasb;
  if constexpr (!SSM) { pwr = lds0 + w * 2048 + lane * 16 + 11 * PITCH; prd = lds0 + r * 2048 + lane * 16 + 11 * PITCH; PIN(pwr);
    biasb = lds0 + TAB + (32 * c + 4 * h) * 4; PIN(biasb); }
  unsigned xw[2][2];
#pragma unroll
  for (int i = 0; i < 2; ++i) { xw[i][0] = lds0 + (xblk[i] & 7) * 1024 + (xblk[i] >> 3) * PITCH + lane * 16; xw[i][1] = xw[i][0] + 8 * PITCH; PIN(xw[i][0]); PIN(xw[i][1]); }
  auto xput = [&](int k) { const int sl = xslot(k % 9); *(LAS u32x4*)(xw[k / 9][sl / 8] + (sl % 8) * PITCH) = xr[k]; };
  u32x2 ones = {0x3f803f80u, 0x3f803f80u}; PIN(ones);
  sfor<0, 2>([&](auto hf_) { constexpr int hf = hf_;
    f32x4 sa[2] = {}, sq[2] = {};
    sfor<0, 3>([&](auto g_) { constexpr int k0 = 9 * hf + 3 * g_;
      wait3<(hf ? 15 - k0 + I0 : 6)>(xr + k0);
      if constexpr (hf == 0) sfor<k0, k0 + 3>([&](auto k_) { gload16<k_ * 128>(xr[9 + k_], voffX, xb + xblk[1] * 9216); });
#pragma unroll
      for (int k = k0; k < k0 + 3; ++k)
#pragma unroll
        for (int e = 0; e < 2; ++e) { const u32x2 v = {xr[k][2 * e], xr[k][2 * e + 1]}; sa[e] = mf4(ones, v, sa[e]); sq[e] = mf4(v, v, sq[e]); } });
    const f32x4 q4 = sq[0] + sq[1];
    float d0 = q4[0], d1 = q4[1], d2 = q4[2], d3 = q4[3]; PIN(d1); PIN(d3);
    d0 = (lane & 1) ? d1 : d0; d2 = (lane & 1) ? d3 : d2;
    float s1 = sa[0][0] + sa[1][0], s2 = (lane & 2) ? d2 : d0;
    s1 += dpp<0xB1>(s1); s2 += dpp<0xB1>(s2); s1 += dpp<0x4E>(s1); s2 += dpp<0x4E>(s2); s1 += dpp<0x141>(s1); s2 += dpp<0x141>(s2);
    const float mean = s1 * (1.f / D);
    const float var = fmaxf(s2 * (1.f / D) - mean * mean, 0.f);
    const float mb = rbf(mean), iden = __builtin_amdgcn_rcpf(rbf(__builtin_amdgcn_sqrtf(rbf(rbf(var) + 1e-5f))));
    const float inv = (SSM ? 1.f : BETA) * iden, ih = rbf(inv), il = rbf(inv - ih), nb = -mb * (ih + il);
    if ((xblk[hf] >> 2) == (jt0 >> 5)) *(LAS f32x2*)(lds0 + FST + ((xblk[hf] & 3) * 8 + (lane >> 3)) * 8) = (f32x2){mb, iden};
    const unsigned bh = __float_as_uint(ih) >> ((lane & 1) ? 0 : 16), bl = __float_as_uint(il) >> ((lane & 1) ? 0 : 16);
    const u32x2 Ah = {(lane & 2) ? 0u : bh, (lane & 2) ? bh : 0u}, Al = {(lane & 2) ? 0u : bl, (lane & 2) ? bl : 0u};
    const f32x4 nv = {nb, nb, nb, nb};
#pragma unroll
    for (int k3 = 9 * hf; k3 < 9 * hf + 9; k3 += 3) {
      f32x4 t[6];
#pragma unroll
      for (int e = 0; e < 6; ++e) t[e] = mf4(Al, (u32x2){xr[k3 + (e >> 1)][2 * (e & 1)], xr[k3 + (e >> 1)][2 * (e & 1) + 1]}, nv);
#pragma unroll
      for (int e = 0; e < 6; ++e) t[e] = mf4(Ah, (u32x2){xr[k3 + (e >> 1)][2 * (e & 1)], xr[k3 + (e >> 1)][2 * (e & 1) + 1]}, t[e]);
#pragma unroll
      for (int e = 0; e < 6; ++e) { xr[k3 + (e >> 1)][2 * (e & 1)] = pk2(t[e][0], t[e][1]); xr[k3 + (e >> 1)][2 * (e & 1) + 1] = pk2(t[e][2], t[e][3]); }
#pragma unroll
      for (int k = k3; k < k3 + 3; ++k) if (k % 9 < NXP) xput(k);
    }
    if constexpr (hf == 0) sfor<0, I0>(issue);
  });
  if (w < 3) {
    PIN(eb2); PIN(edk);
    *(LAS unsigned*)(lds0 + ETAB + min(tid, 143) * 4) = eb2 | (edk << 16);
    if constexpr (SSM) {
      PIN(plr); PIN(pli); PIN(pdt);
      if (tid < NSIG) {
        const float lr = lo2f(plr), li = lo2f(pli), dt = rbf(expf(lo2f(pdt)));
        const float e = expf(dt * lr); float sn, cs; sincosf(dt * li, &sn, &cs);
        const float ar = e * cs, ai = e * sn, nr = ar - 1.f, den = lr * lr + li * li;
        const float k2 = (sg >= 2 && tid >= 128) ? 0.f : 2.f / den;
        *(LAS f32x4*)(lds0 + TAB + tid * 16) = (f32x4){ar, ai, (nr * lr + ai * li) * k2, (ai * lr - nr * li) * k2};
        const cf a8 = csq(csq(csq(cf{ar, ai}))), a16 = csq(a8);
        *(LAS f32x4*)(lds0 + TAB1 + tid * 16) = (f32x4){a8.r, a8.i, a16.r, a16.i};
      }
    } else {
      PIN(braw);
      if (tid < 96) *(LAS f32x4*)(lds0 + TAB + tid * 16) = (f32x4){BETA * lo2f(braw[0]), BETA * hi2f(braw[0]), BETA * lo2f(braw[1]), BETA * hi2f(braw[1])};
    }
  }
  asm volatile("" : "+s"(rtag), "+s"(tag));
  wait_bar<S.vm[0], 0>(); TS(3);
  constexpr int PF = SSM ? 3 : PFD, NB = PF + 1;
  bf16x8 af[NB];
  f32x16 acc, prev, oacc[9];
  u32x4 P[6][4], pn[2], nbias[4];
  af[0] = fread(ic<0>{}); xread(0);
  if constexpr (!SSM) {
#pragma unroll
    for (int g = 0; g < 4; ++g) nbias[g] = lds128(biasb + g * 32); }
  af[1] = fread(ic<1>{}); xread(1); af[2] = fread(ic<2>{}); xread(2);
  if constexpr (PF == 4) af[3] = fread(ic<3>{});
  cf u[NCH][8];
  const f32x16 zero16 = {};
  float gt0, gt1;
  char* pbase; unsigned pvo, jvo;
  auto gelu_ph = [&](int e, int ph) {
    const float v0 = prev[2 * e], v1 = prev[2 * e + 1];
    if (ph == 0) { gt0 = v0 * __builtin_fmaf(v0, v0, KAP); gt1 = v1 * __builtin_fmaf(v1, v1, KAP); PIN(gt0); PIN(gt1); }
    if (ph == 1) { gt0 = __builtin_amdgcn_exp2f(-gt0); gt1 = __builtin_amdgcn_exp2f(-gt1); PIN(gt0); PIN(gt1); }
    if (ph == 2) { gt0 = __builtin_amdgcn_rcpf(1.f + gt0); gt1 = 1.f + gt1; PIN(gt0); PIN(gt1); }
    if (ph == 3) { unsigned pv = pk2(v0 * gt0, v1 * __builtin_amdgcn_rcpf(gt1)); PIN(pv); pn[e >> 2][e & 3] = pv; }
  };
  cf sE, sH, sU;
  f32x4 stab, stab1;
  unsigned segw;
#define PINC(z) do { if (pin) { PIN((z).r); PIN((z).i); } } while (0)
  auto pass1 = [&](int ch, int k, bool pin) {
    if (k == 0) { int tz = threadIdx.x; PIN(tz); segw = lds0 + (32 * ch + 16 * c + (tz & 15)) * 8;
      stab = *(LAS f32x4*)(2 * segw - lds0 + TAB); }
    const cf av = {stab[0], stab[1]};
    if (k >= 1 && k <= 4) { for (int j = 2 * k - 2; j < 2 * k; ++j) { swap16(prev[j], prev[j + 8], u[ch][j].r, u[ch][j].i); PINC(u[ch][j]); } }
    if (k >= 5 && k <= 11) { const int i = k - 5; sE = cmad(av, i ? sE : u[ch][0], u[ch][i + 1]); PINC(sE); }
    if (k == 9) stab1 = *(LAS f32x4*)(2 * segw - lds0 + TAB1);
    const cf a8 = {stab1[0], stab1[1]}, a16 = {stab1[2], stab1[3]};
    if (k == 12) { swap16(sE.r, sE.r, sE.r, sH.r); swap16(sE.i, sE.i, sE.i, sH.i); PINC(sH); PINC(sE); }
    if (k == 13) { sU = cmad(a8, sE, sH); PINC(sU); }
    if (k == 14) { swap32(sU.r, sU.r, sH.r); swap32(sU.i, sU.i, sH.i); PINC(sH); PINC(sU); }
    if (k == 15) { cf Sg = cmad(a16, sU, sH); PINC(Sg); *(LAS f32x2*)(segw + SEG + r * NSIG * 8) = (f32x2){Sg.r, Sg.i}; }
    if (k == 16) { sU = {selm(0.f, sU.r, HMASK), selm(0.f, sU.i, HMASK)}; sE = cmad(a8, sU, sE); PINC(sU); PINC(sE); }
    if (k == 17) { u[ch][0] = cmad(av, cf{selm(sU.r, sE.r, GMASK), selm(sU.i, sE.i, GMASK)}, u[ch][0]); PINC(u[ch][0]); }
    if (k >= 18) { const int j = k - 18; u[ch][j] = cmul(cf{stab[2], stab[3]}, u[ch][j]); PINC(u[ch][j]); }
  };
  char* const granb = a.ws + WS_GRAN + (size_t)(sg * 16 + 15 - rb) * (NSIG * 16);
  auto seg_rd = [&](int t, cf* sv) {
#pragma unroll
    for (int i = 0; i < 4; ++i) { const f32x2 v = *(LAS f32x2*)(lds0 + SEG + (i * NSIG + t) * 8); sv[i] = {v[0], v[1]}; } };
  auto pow32 = [&](int t) { const f32x2 v = *(LAS f32x2*)(lds0 + TAB1 + t * 16 + 8); return csq(cf{v[0], v[1]}); };
  auto carry_out = [&](int t, cf a32) { cf sv[4]; seg_rd(t, sv);
    const cf T = cmad(a32, cmad(a32, cmad(a32, sv[0], sv[1]), sv[2]), sv[3]);
    __builtin_amdgcn_raw_buffer_store_b128((u32x4){__float_as_uint(T.r), tag, __float_as_uint(T.i), tag}, __builtin_amdgcn_make_buffer_rsrc(granb, 0, NSIG * 16, 0x00020000), t * 16, 0, 16); };
  auto carry_ld = [&](int t, u32x4* v) {
    const auto rs = __builtin_amdgcn_make_buffer_rsrc(granb + NSIG * 16, 0, rb * (NSIG * 16), 0x00020000);
    unsigned vo = t * 16; PIN(vo);
#pragma unroll
    for (int k = 0; k < 15; ++k) v[k] = __builtin_amdgcn_raw_buffer_load_b128(rs, vo + k * (NSIG * 16), 0, 16); };
  auto carry_in = [&](int t, cf a32, u32x4* v) {
    cf sv[4]; seg_rd(t, sv);
    const cf a128 = csq(csq(a32)), a512 = csq(csq(a128));
    for (unsigned spins = 0; spins < (1u << 16); ++spins) {
      unsigned sum = 0;
#pragma unroll
      for (int k = 0; k < 15; ++k) sum += v[k][1] + v[k][3];
      if (__all(sum == 2u * rb * tag)) break;
      carry_ld(t, v);
    }
    cf q[4];
#pragma unroll
    for (int i = 3; i >= 0; --i)
#pragma unroll
      for (int n = 0; n < 4; ++n) { const int k = 4 * n + i; if (k > 14) continue;
        const cf x = {__uint_as_float(v[k][0]), __uint_as_float(v[k][2])}; q[n] = (i == 3 || k == 14) ? x : cmad(a128, q[n], x); }
    cf G = cmad(a512, cmad(a512, cmad(a512, q[3], q[2]), q[1]), q[0]);
#pragma unroll
    for (int i = 0; i < 4; ++i) { *(LAS f32x2*)(lds0 + SEG + (i * NSIG + t) * 8) = (f32x2){G.r, G.i}; G = cmad(a32, G, sv[i]); } };
  unsigned hr[2][2];
  auto hread = [&](int n) { const int ch = h_ch(n), kk = h_kk(n); P[ch][kk] = __builtin_bit_cast(u32x4, cat(trd(hr[ch >> 2][0] + (ch & 3) * 16384 + kk * 1024), trd(hr[ch >> 2][1] + (ch & 3) * 16384 + kk * 1024))); };
  auto ssm_boundary = [&]() {
    tid = threadIdx.x; PIN(tid); lane = tid & 63; h = lane >> 5;
    const int ts = min(tid, NSIG - 1);
    u32x4 v[15];
    if (w < 2) carry_ld(ts, v);
    const cf a32 = pow32(ts);
#pragma unroll
    for (int k = 0; k < 16; ++k) pass1(NCH - 1, k, false);
    const int g = (lane >> 4) & 1, s16 = lane & 15;
#pragma unroll
    for (int k = 0; k < 4 * PARK; ++k) { const f32x4 t = *(LAS f32x4*)(lds0 + KSLOT * PITCH + tid * 16 + k * 8192); u[0][2 * k] = {t[0], t[1]}; u[0][2 * k + 1] = {t[2], t[3]}; }
    SBAR();
    if (w < 2) carry_in(ts, a32, v);
    wait_bar<63, 0>();
    if (w == 2) carry_out(ts, a32);
    const int q4 = (lane & 15) >> 2, pq = lane & 3;
    unsigned hw[2];
    hw[0] = lds0 + HSLOT * PITCH + r * 4096 + (16 * c + s16) * 64 + (((2 * h + g) ^ ((s16 >> 1) & 3)) << 4); hw[1] = hw[0] + 65536;
    PIN(hw[0]); PIN(hw[1]);
#pragma unroll
    for (int k = 0; k < 2; ++k) {
      hr[0][k] = lds0 + HSLOT * PITCH + r * 4096 + (8 * h + 4 * k + q4) * 64 + (((2 * g + (pq >> 1)) ^ (2 * k + (q4 >> 1))) << 4) + (pq & 1) * 8; hr[1][k] = hr[0][k] + 65536;
      PIN(hr[0][k]); PIN(hr[1][k]); }
    cf av[NCH], pw[NCH];
    auto prep = [&](int ch) {
      const f32x4 t = *(LAS f32x4*)(lds0 + TAB + (32 * ch + 16 * c + s16) * 16), t1 = *(LAS f32x4*)(lds0 + TAB1 + (32 * ch + 16 * c + s16) * 16);
      av[ch] = {t[0], t[1]};
      pw[ch] = cmul(cmul(cf{t[2], t[3]}, cf{selm(1.f, t1[0], GMASK), selm(0.f, t1[1], GMASK)}), cf{selm(1.f, t1[2], HMASK), selm(0.f, t1[3], HMASK)}); };
    auto fin = [&](int c0, int c1) {
      f32x2 z[NCH], o[NCH][8];
#pragma unroll
      for (int ch = c0; ch < c1; ++ch) { const f32x2 ci = *(LAS f32x2*)(lds0 + SEG + (r * NSIG + 32 * ch + 16 * c + s16) * 8);
        const cf z0 = cmul(pw[ch], cf{ci[0], ci[1]}); z[ch] = (f32x2){z0.r, z0.i}; }
#pragma unroll
      for (int i = 0; i < 8; ++i)
#pragma unroll
        for (int ch = c0; ch < c1; ++ch) {
          const f32x2 t = __builtin_elementwise_fma((f32x2){av[ch].r, av[ch].r}, z[ch], (f32x2){u[ch][i].r, u[ch][i].i});
          z[ch] = __builtin_elementwise_fma((f32x2){-av[ch].i, av[ch].i}, __builtin_shufflevector(z[ch], z[ch], 1, 0), t); o[ch][i] = z[ch]; }
#pragma unroll
      for (int ch = c0; ch < c1; ++ch) {
        *(LAS u32x4*)(hw[ch >> 2] + (ch & 3) * 16384) = (u32x4){pk2(o[ch][0][0], o[ch][1][0]), pk2(o[ch][2][0], o[ch][3][0]), pk2(o[ch][4][0], o[ch][5][0]), pk2(o[ch][6][0], o[ch][7][0])};
        *(LAS u32x4*)(hw[ch >> 2] + (ch & 3) * 16384 + 2048) = (u32x4){pk2(-o[ch][0][1], -o[ch][1][1]), pk2(-o[ch][2][1], -o[ch][3][1]), pk2(-o[ch][4][1], -o[ch][5][1]), pk2(-o[ch][6][1], -o[ch][7][1])};
      } };
#pragma unroll
    for (int ch = 0; ch < NCH; ++ch) prep(ch);
    fin(0, 4);
    SBAR();
    if (w == 2) carry_ld(ts, v);
#pragma unroll
    for (int k = 16; k < 26; ++k) pass1(NCH - 1, k, false);
    SBAR();
    if (w == 2) carry_in(ts, a32, v);
    wait_bar<63, 0>();
    if (rb == 15 && tid == 0) __hip_atomic_store((gu32*)(a.ws + WS_EPOCH + sg * 256), tag, RLX_AGENT);
#pragma unroll
    for (int n = 0; n < 16; ++n) hread(n);
    fin(4, 5);
  };
  unsigned pfo, pf1 = min(lane, 8) * (SSM ? 128 : 9216);
  { constexpr int LP = 432 / NSH / NPF;
    const int ln = min(lane / TG, LP - 1), sub = (lane % TG) * 64, l3 = ln / (LP / 3);
    pfo = sub + (SSM ? (ln % (LP / 3)) * 1152 + (l3 < 2 ? p0 * 2 + l3 * 128 : 1024) : ln * 128); PIN(pfo); }
  char* const flagb = a.ws + WS_FLAG + rb * 4096;
  u32x2 fl;
  auto publish = [&](unsigned n) { flag_store(flagb + slice * 256, n, rtag, w); };
  auto await = [&](u32x2 v, unsigned n0, unsigned n1) {
    const unsigned need = (pvo & 512) ? n1 : n0;
    if (__builtin_expect(!__all(need == 0 || (v[1] == rtag && v[0] >= need)), 0)) {
      for (unsigned spins = 0; spins < (1u << 16); ++spins) {
        const unsigned long long t = __hip_atomic_load((gu64*)(flagb + w * 256 + (pvo & 512) * 4), RLX_AGENT);
        if (__all(need == 0 || ((unsigned)(t >> 32) == rtag && (unsigned)t >= need))) break;
        __builtin_amdgcn_s_sleep(4);
      }
    } };
  const char* const jsrc = a.ws + WS_PART + (size_t)(w * 16 + rb) * PART_BLK + slice * 1024;
  auto jissue = [&](auto j_) { constexpr int j = j_, so = S.jslot[j] * PITCH;
    dma1s(jvo, jsrc + (j >> 1) * 16384 + (size_t)(j & 1) * (128 * PART_BLK), lds0 + w * 1024 + so); };
  sfor<0, C::NM / 4>([&](auto p_) { sfor<4 * p_, 4 * p_ + 4>([&](auto m_) {
    constexpr int m = m_;
    constexpr bool ph1 = m < C::NM1;
    constexpr int un = m / 36, gn = m % 36, m2 = m - C::NM1;

    if constexpr (m == C::NM1) { TS(5);
      if constexpr (SSM) ssm_boundary();
      int tz = threadIdx.x; PIN(tz);
      pvo = (tz & 63) * 16; jvo = pvo ^ (r << 5) ^ ((tz & 32) << 2); pbase = a.ws + WS_PART + (size_t)(slice * 16 + rb) * PART_BLK + w * 2048; }
    if constexpr (!SSM && m == 180) { PIN(fb2[0]); PIN(fb2[1]); PIN(fb2[2]); PIN(prd); }
    if constexpr (m > 0 && bar_at(m)) {
      constexpr bool lgk0 = (FINE == 24 && m == 24) || (SSM ? m == C::NM1 || m == C::NM1 - 24 : m == 204 || m == 252);
      wait_bar<S.vm[m], (lgk0 ? 0 : 15)>();
      if constexpr (pub_at<SSM>(m)) { constexpr int pc = pub_count<SSM>(m); publish(pc); }
      if constexpr (chk_at<SSM>(m)) { constexpr int n0 = N0[chk_at<SSM>(m)], n1 = N1[chk_at<SSM>(m)]; pin2(fl); await(fl, n0, n1); }
    }
    constexpr bool post = m >= 36 && (ph1 || (!SSM && m2 < 36));
    if constexpr (m + PF < C::NM) af[(m + PF) % NB] = fread(ic<m + PF>{});
    if constexpr (m + 3 < 36) xread(m + 3);
    if constexpr (SSM && m2 >= 0 && m2 < 4) hread(m2 + 16);
    if constexpr (!SSM) {
      if constexpr (m >= 192 && m < 204) { constexpr int n = m - 192; P[n >> 2][n & 3] = lds128(prd + (pslot(n >> 2) - 11) * PITCH + ((n & 3) >> 1) * PITCH + (n & 1) * 1024); }
      if constexpr (m >= 204 && m < 208) { constexpr int n = m - 204; P[3][n] = lds128(prd + (pslot(3) - 11) * PITCH + (n >> 1) * PITCH + (n & 1) * 1024); }
      if constexpr (m >= 228 && m < 232) { constexpr int n = m - 228; P[4][n] = lds128(prd + (pslot(4) - 11) * PITCH + (n >> 1) * PITCH + (n & 1) * 1024); }
      if constexpr (m >= 252 && m < 256) { constexpr int n = m - 252; P[5][n] = lds128(prd + (pslot(5) - 11) * PITCH + (n >> 1) * PITCH + (n & 1) * 1024); }
      if constexpr (ph1 && un < 5 && gn >= 30 && gn < 34) nbias[gn - 30] = lds128(biasb + (un + 1) * 256 + (gn - 30) * 32);
    }
    if constexpr (ph1) {
      if constexpr (SSM) acc = MFMA(__builtin_bit_cast(bf16x8, xf[gn]), af[m % NB], gn == 0 ? zero16 : acc);
      else {
        if constexpr (gn == 0) {
#pragma unroll
          for (int e = 0; e < 16; ++e) acc[e] = __uint_as_float(nbias[e >> 2][e & 3]); }
        acc = MFMA(af[m % NB], __builtin_bit_cast(bf16x8, xf[gn]), acc); }
    } else {
      constexpr int dp = p2_dp<SSM>(m2 / 4), ch = p2_ch<SSM>(m2 / 4);
      constexpr int bc = SSM ? h_ch(m2 % C::UNIT2) : ch, bk = SSM ? h_kk(m2 % C::UNIT2) : m & 3;
      oacc[dp] = MFMA(af[m % NB], __builtin_bit_cast(bf16x8, P[bc][bk]), (ch == 0 && (m & 3) == 0) ? zero16 : oacc[dp]);
    }
    SBAR();
    if constexpr (m >= 4 * NXP - 12 && m < 24 && (m & 1) == 0) xput(3 + (m >> 2) + 9 * ((m >> 1) & 1));
    if constexpr (ph1 && gn == 35) prev = acc;
    if constexpr (SSM) {
      if constexpr (PARK && m == 72) { int tz = threadIdx.x; PIN(tz);
#pragma unroll
        for (int k = 0; k < 4; ++k) *(LAS f32x4*)(lds0 + KSLOT * PITCH + tz * 16 + k * 8192) = (f32x4){u[0][2 * k].r, u[0][2 * k].i, u[0][2 * k + 1].r, u[0][2 * k + 1].i}; }
      if constexpr (post && gn < 35) {
        constexpr int k0 = un < NCH - 1 ? gn * 26 / 35 : gn < 12 ? gn * 16 / 12 : 16 + (gn - 12) * 10 / 23, k1 = un < NCH - 1 ? (gn + 1) * 26 / 35 : gn < 11 ? (gn + 1) * 16 / 12 : 16 + (gn - 11) * 10 / 23;
        sfor<k0, k1>([&](auto k_) { pass1(un - 1, k_, true); }); }
      if constexpr (m == C::NM1 - 24) { int tz = threadIdx.x; PIN(tz); if (w < 2) carry_out(tz, pow32(tz)); } }
    else {
      if constexpr (m >= 72 && m <= 217 && gn < 2) *(LAS u32x4*)(pwr + (pslot(un - 2) - 11) * PITCH + gn * 1024) = pn[gn];
      if constexpr (post && gn >= 2 && gn < 34) gelu_ph((gn - 2) >> 2, (gn - 2) & 3);
      if constexpr (m2 == 20 || m2 == 34) *(LAS u32x4*)(pwr + (pslot(5) - 11) * PITCH + (m2 == 34) * 1024) = pn[m2 == 34];
    }
    if constexpr (store_of<SSM>(m) >= 0) {
      constexpr int dp = store_of<SSM>(m) >> 1, e = (store_of<SSM>(m) & 1) * 8;
      u32x4 o = {pk2(oacc[dp][e], oacc[dp][e + 1]), pk2(oacc[dp][e + 2], oacc[dp][e + 3]), pk2(oacc[dp][e + 4], oacc[dp][e + 5]), pk2(oacc[dp][e + 6], oacc[dp][e + 7])};
      pst(pvo, pbase + dp * 16384 + (e >> 3) * 1024, o);
    }
    if constexpr (pf_of(m) >= 0) {
      const char* sb;
      if constexpr (SSM) { sb = sb2 + (((rb & (NSH - 1)) * 4 + r) * NPF + pf_of(m)) * (144 / NSH / NPF) * 1152; }
      else sb = sb2 + (((rb & (NSH - 1)) * 8 + w) * NPF + pf_of(m)) * (432 / NSH / NPF) * 128;
      touch(pfo, sb, lds0 + LDS_BYTES - 256);
    }
    if constexpr (pf1_of(m) >= 0) {
      constexpr int k = pf1_of(m);
      if constexpr (SSM) { if constexpr (k < 4) touch(pf1, (const char*)(c ? a.bim : a.bre) + (size_t)((k < 3 ? p0 + 32 * (k + 1) : p4) + (rb & 7) * 4 + r) * 1152, lds0 + LDS_BYTES - 256); else touch(pf1, sb1, lds0 + LDS_BYTES - 256); }
      else touch(pf1, sb1 + (k + 1) * 128 + (size_t)((rb & 7) * 72 + w * 9) * 9216, lds0 + LDS_BYTES - 256);
    }
    if constexpr (xld_at<SSM>(m) >= 0) { int tz = threadIdx.x; PIN(tz); touch(min((tz >> 5) * 128, CODEPF / 32 - 128), code, lds0 + LDS_BYTES - 256); }
    if constexpr (fld_at<SSM>(m)) gload8s(fl, (pvo & 512) * 4, flagb + w * 256);
    if constexpr (job_at<SSM>(m) >= 0) jissue(ic<JORD[job_at<SSM>(m) < 0 ? 0 : job_at<SSM>(m)]>{});
    sfor<S.fis[m], S.fis[m] + S.nis[m]>(issue);
    SBAR();
  }); });
  TS(13);
#pragma unroll
  for (int hf = 0; hf < 2; ++hf) { const int e = hf * 8;
    u32x4 o = {pk2(oacc[8][e], oacc[8][e + 1]), pk2(oacc[8][e + 2], oacc[8][e + 3]), pk2(oacc[8][e + 4], oacc[8][e + 5]), pk2(oacc[8][e + 6], oacc[8][e + 7])};
    pst(pvo, pbase + 8 * 16384 + hf * 1024, o); }
  gload8s(fl, (pvo & 512) * 4, flagb + w * 256);
  float* const outb = kargs()->out + (size_t)t0 * D;
  tid = threadIdx.x; PIN(tid); lane = tid & 63;
  unsigned eo = ((jt0 + (tid >> 4)) * D + jd0 + (tid & 15)) * 2, eo4 = eo * 2; PIN(eo); PIN(eo4);
  const f32x2 stt = *(LAS f32x2*)(lds0 + FST + (tid >> 4) * 8);
  float xv[9], eb[9], ed[9]; unsigned ja[9], je[9];
  u32x4 sel[2];
#pragma unroll
  for (int b = 0; b < 2; ++b) { const float o = __uint_as_float(0x3f803f80u);
    const unsigned lo = __float_as_uint(selm(0.f, o, 0x100000001ull << (4 * b))), hi = __float_as_uint(selm(0.f, o, 0x200000001ull << (4 * b))); sel[b] = (u32x4){lo, lo, hi, hi}; PIN(sel[b]); }
  const unsigned jb = ((lane & 15) >> 2) * 1024 + (((4 * w + ((lane >> 4) & 1) + 32 * (lane & 1)) ^ ((lane & 12) >> 1) ^ ((lane & 1) << 3)) << 4) + ((lane >> 1) & 1) * 8;
  sfor<0, 9>([&](auto dp_) { constexpr int dp = dp_, s0 = S.jslot[2 * dp] * PITCH, s1 = S.jslot[2 * dp + 1] * PITCH;
    const unsigned so = lds0 + s0 + (lane >> 5) * (s1 - s0); ja[dp] = so + jb; je[dp] = so + (jb ^ 32); pin1(ja[dp]); pin1(je[dp]);
    const unsigned e = *(LAS unsigned*)(lds0 + ETAB + (dp * 16 + (lane & 15)) * 4); eb[dp] = lo2f(e); ed[dp] = hi2f(e); pinf(eb[dp]); pinf(ed[dp]); xv[dp] = 0.f; pinf(xv[dp]); });
  wait_bar<0, 0>();
  publish(9);
  TS(7);
  pin2(fl); await(fl, MAXPUB, MAXPUB);
  sfor<0, 9>([&](auto dp_) { gload_hi<dp_ * 128>(xv[dp_], eo, xb); });
  sfor<NJIN, 2 * MAXPUB>([&](auto k_) { jissue(ic<JORD[k_]>{}); });
  asm volatile("s_waitcnt vmcnt(%0)" :: "n"(2 * MAXPUB - NJIN) : "memory");
#pragma unroll
  for (int dp = 0; dp < 9; ++dp) { PIN(xv[dp]); ed[dp] = rbf(rbf(rbf(xv[dp] - stt[0]) * stt[1]) * ed[dp]); PIN(ed[dp]); }
  auto finish = [&](auto d0_, auto d1_) { constexpr int d0 = d0_, d1 = d1_;
    bf16x8 b0[9], b1[9]; f32x16 t[9]; float uu[9], ee[9];
#pragma unroll
    for (int dp = d0; dp < d1; ++dp) { b0[dp] = cat(trd(ja[dp]), trd(ja[dp] + 4096)); b1[dp] = cat(trd(je[dp]), trd(je[dp] + 4096)); }
    SBAR();
#pragma unroll
    for (int dp = d0; dp < d1; ++dp) t[dp] = MFMA(__builtin_bit_cast(bf16x8, sel[0]), b0[dp], zero16);
#pragma unroll
    for (int dp = d0; dp < d1; ++dp) t[dp] = MFMA(__builtin_bit_cast(bf16x8, sel[1]), b1[dp], t[dp]);
    SBAR();
#pragma unroll
    for (int dp = d0; dp < d1; ++dp) uu[dp] = rbf(rbf(t[dp][0] * IBETA) + eb[dp]);
#pragma unroll
    for (int dp = d0; dp < d1; ++dp) ee[dp] = __builtin_amdgcn_exp2f(uu[dp] * -1.4426950409f);
#pragma unroll
    for (int dp = d0; dp < d1; ++dp) ee[dp] = __builtin_amdgcn_rcpf(1.f + ee[dp]);
    sfor<d0, d1>([&](auto dp_) { constexpr int dp = dp_; gstore4<dp * 256>(eo4, outb, xv[dp] + (t[dp][1] + ed[dp]) * rbf(uu[dp] * ee[dp])); });
    SBAR(); };
  constexpr int F0 = NJIN >= 8 ? NJIN / 2 : NJIN >= 7 ? 3 : 0;
  finish(ic<0>{}, ic<F0>{});
  await((u32x2){0u, 0u}, 9, 9);
  TS(10);
  sfor<2 * MAXPUB, 18>([&](auto k_) { jissue(ic<JORD[k_]>{}); });
  wait_bar<18 - 2 * MAXPUB, 15>();
  flag_store(a.ws + WS_REPOCH + rb * 256, rtag, rtag, w | slice);
  finish(ic<F0>{}, ic<MAXPUB>{});
  wait_bar<MAXPUB - F0, 15>();
  finish(ic<MAXPUB>{}, ic<9>{});
  TS(11);
}
#undef S
__global__ __launch_bounds__(512, 2) void k_main(Args a) {
  __shared__ __attribute__((aligned(16))) char smem[LDS_BYTES];
  const unsigned lds0 = (unsigned)(uintptr_t)smem;
  const int bid = blockIdx.x, xcd = bid & 7, loc = bid >> 3;
  const int slice = (loc & 3) * 4 + (xcd & 3), rb = (xcd >> 2) * 8 + (loc >> 2);
  const char* pc; asm volatile("s_getpc_b64 %0" : "=s"(pc));
  pc += loc * (CODEPF / 32);
  if (slice >= NFFN) {
    const int w = __builtin_amdgcn_readfirstlane(threadIdx.x >> 6);
    touch(min((int)threadIdx.x & 63, 35) * 128, (const char*)a.x + (size_t)(rb * 128 + 96) * 1152 + w * 4608, lds0 + LDS_BYTES - 256); }
  asm volatile("" ::: "memory");
  if (slice < NFFN) body<false>(lds0, slice, rb, pc); else body<true>(lds0, slice, rb, pc);
}

extern "C" void kernel_launch(void* const* d_in, const int* in_sizes, int n_in, void* d_out, int out_size, void* d_ws, size_t ws_size, hipStream_t stream) {
  Args a;
  a.x = (const u16*)d_in[0]; a.lre = (const u16*)d_in[1]; a.lim = (const u16*)d_in[2]; a.bre = (const u16*)d_in[3]; a.bim = (const u16*)d_in[4];
  a.cre = (const u16*)d_in[5]; a.cim = (const u16*)d_in[6]; a.ldt = (const u16*)d_in[7]; a.dsk = (const u16*)d_in[8];
  a.w1 = (const u16*)d_in[10]; a.b1 = (const u16*)d_in[11]; a.w2 = (const u16*)d_in[12]; a.b2 = (const u16*)d_in[13];
  a.ws = (char*)d_ws; a.out = (float*)d_out;
  k_main<<<256, 512, 0, stream>>>(a);
}
#define NSTAMP 16
#define STAMP_VALID(b) 1
#define STAMP_CLASS(b) ((((b) >> 3) & 3) == 3)
```

```cpp
#include <hip/hip_runtime.h>
#include <stdint.h>
#include <type_traits>

typedef unsigned short u16;
typedef short bf16x8 __attribute__((ext_vector_type(8)));
typedef short s16x4 __attribute__((ext_vector_type(4)));
typedef float f32x16 __attribute__((ext_vector_type(16)));
typedef float f32x4 __attribute__((ext_vector_type(4)));
typedef float f32x2 __attribute__((ext_vector_type(2)));
typedef unsigned u32x4 __attribute__((ext_vector_type(4)));
typedef unsigned u32x2 __attribute__((ext_vector_type(2)));
typedef __bf16 bf2 __attribute__((ext_vector_type(2)));
#define LAS __attribute__((address_space(3)))
#define GAS __attribute__((address_space(1)))
#define DI __device__ __forceinline__
typedef GAS unsigned gu32;
typedef GAS unsigned long long gu64;
#define RLX_AGENT __ATOMIC_RELAXED, __HIP_MEMORY_SCOPE_AGENT
template <int V> using ic = std::integral_constant<int, V>;

constexpr int D = 576;
constexpr int NFFN = 12, NSL = 16, FS = 384, NCH = 5, NSIG = 32 * NCH;
constexpr int LDS_BYTES = 163840;
constexpr int CODE_STRIDE = 1872;
constexpr size_t PART_BLK = 9 * 8 * 2048;
constexpr size_t WS_PART = 0;
constexpr size_t WS_EPOCH = WS_PART + (size_t)NSL * 16 * PART_BLK;
constexpr size_t WS_GRAN = WS_EPOCH + 1024;
constexpr size_t WS_REPOCH = WS_GRAN + 4 * 16 * 2 * NSIG * 8;
constexpr size_t WS_FLAG = WS_REPOCH + 16 * 256;
constexpr float BETA = 0.46866869f;
constexpr float KAP = 4.9122295f;
constexpr float IBETA = 1.0f / BETA;

#define SBAR() __builtin_amdgcn_sched_barrier(0)
#define PIN(x) asm volatile("" : "+v"(x))
#define MFMA(a, b, c) __builtin_amdgcn_mfma_f32_32x32x16_bf16(a, b, c, 0, 0, 0)

DI float lo2f(unsigned v) { return __uint_as_float(v << 16); }
DI float hi2f(unsigned v) { return __uint_as_float(v & 0xffff0000u); }
DI unsigned pk2(float a, float b) { bf2 r = {(__bf16)a, (__bf16)b}; return __builtin_bit_cast(unsigned, r); }
DI float rbf(float a) { return (float)(__bf16)a; }
DI void dma1(unsigned voff, const char* sb, unsigned dst) {
  asm volatile("s_mov_b32 m0, %2\n\ts_nop 0\n\tglobal_load_lds_dwordx4 %0, %1" :: "v"(voff), "s"(sb), "s"(dst) : "memory");
}
template <int I, int N, class Fn> DI void sfor(Fn&& f) { if constexpr (I < N) { f(ic<I>{}); sfor<I + 1, N>(f); } }
template <int V, int G> DI void wait_bar() { SBAR(); asm volatile("s_waitcnt vmcnt(%0) lgkmcnt(%1)\n\ts_barrier" :: "n"(V), "n"(G) : "memory"); SBAR(); }
DI s16x4 trd(unsigned a) { return __builtin_amdgcn_ds_read_tr16_b64_v4i16((LAS s16x4*)a); }
DI bf16x8 cat(s16x4 lo, s16x4 hi) { return __builtin_shufflevector(lo, hi, 0, 1, 2, 3, 4, 5, 6, 7); }
DI u32x4 lds128(unsigned a) { return *(LAS u32x4*)a; }
DI void pst(unsigned voff, char* sb, u32x4 v) { asm volatile("global_store_dwordx4 %0, %1, %2 sc1\n\ts_nop 1" :: "v"(voff), "v"(v), "s"(sb) : "memory"); }
template <int OFF> DI void gload16(u32x4& d, unsigned voff, const char* sb) { asm volatile("global_load_dwordx4 %0, %1, %2 offset:%3" : "=v"(d) : "v"(voff), "s"(sb), "n"(OFF) : "memory"); }
template <int N> DI void wait3(u32x4* v) { asm volatile("s_waitcnt vmcnt(%3)" : "+v"(v[0]), "+v"(v[1]), "+v"(v[2]) : "n"(N) : "memory"); }
DI f32x4 mf4(u32x2 x, u32x2 y, f32x4 z) { return __builtin_amdgcn_mfma_f32_4x4x4bf16_1k(__builtin_bit_cast(s16x4, x), __builtin_bit_cast(s16x4, y), z, 0, 0, 0); }
DI void pin1(unsigned& v) { PIN(v); }
DI void dma1s(unsigned voff, const char* sb, unsigned dst) {
  asm volatile("s_mov_b32 m0, %2\n\ts_nop 0\n\tglobal_load_lds_dwordx4 %0, %1 sc1" :: "v"(voff), "s"(sb), "s"(dst) : "memory");
}
DI void gload8s(u32x2& d, unsigned voff, const char* sb) { asm volatile("global_load_dwordx2 %0, %1, %2 sc1" : "=v"(d) : "v"(voff), "s"(sb) : "memory"); }
DI void pin2(u32x2& v) { PIN(v); }
DI void flag_store(char* p, unsigned n, unsigned tag, int w) {
  u32x2 v = {n, tag}; unsigned z = 0; unsigned long long keep;
  asm volatile("s_cmp_lg_u32 %4, 0\n\ts_cbranch_scc1 1f\n\ts_mov_b64 %0, exec\n\ts_mov_b64 exec, 1\n\tglobal_store_dwordx2 %1, %2, %3 sc1\n\ts_mov_b64 exec, %0\n1:"
               : "=&s"(keep) : "v"(z), "v"(v), "s"(p), "s"(w) : "memory", "scc");
}
constexpr unsigned long long GMASK = 0xFFFF0000FFFF0000ull, HMASK = 0xFFFFFFFF00000000ull;
DI float selm(float a, float b, unsigned long long m) { float r; asm("v_cndmask_b32 %0, %1, %2, %3" : "=v"(r) : "v"(a), "v"(b), "s"(m)); return r; }
DI void touch(unsigned voff, const char* sb, unsigned dst) { asm volatile("s_mov_b32 m0, %2\n\ts_nop 0\n\tglobal_load_lds_dword %0, %1" :: "v"(voff), "s"(sb), "s"(dst) : "memory"); }
template <int OFF> DI void gload_hi(float& d, unsigned voff, const char* sb) { asm volatile("global_load_short_d16_hi %0, %1, %2 offset:%3" : "+v"(d) : "v"(voff), "s"(sb), "n"(OFF) : "memory"); }
template <int OFF> DI void gstore4(unsigned voff, float* sb, float v) { asm volatile("global_store_dword %0, %1, %2 offset:%3 sc1" :: "v"(voff), "v"(v), "s"(sb), "n"(OFF) : "memory"); }
DI void pinf(float& v) { PIN(v); }
template <int CTRL> DI float dpp(float v) { return __int_as_float(__builtin_amdgcn_update_dpp(0, __float_as_int(v), CTRL, 0xf, 0xf, true)); }
struct cf { float r, i; };
DI cf cmul(cf a, cf b) { return {__builtin_fmaf(a.r, b.r, -a.i * b.i), __builtin_fmaf(a.r, b.i, a.i * b.r)}; }
DI cf csq(cf a) { return {__builtin_fmaf(a.r, a.r, -a.i * a.i), (a.r + a.r) * a.i}; }
DI cf cmad(cf a, cf b, cf c) { return {__builtin_fmaf(a.r, b.r, __builtin_fmaf(-a.i, b.i, c.r)), __builtin_fmaf(a.r, b.i, __builtin_fmaf(a.i, b.r, c.i))}; }
DI void swap32(float x, float& lo, float& hi) { auto r = __builtin_amdgcn_permlane32_swap(__float_as_uint(x), __float_as_uint(x), false, false); lo = __uint_as_float(r[0]); hi = __uint_as_float(r[1]); }
DI void swap16(float x, float y, float& a, float& b) { auto r = __builtin_amdgcn_permlane16_swap(__float_as_uint(x), __float_as_uint(y), false, false); a = __uint_as_float(r[0]); b = __uint_as_float(r[1]); }

template <bool SSM> struct Cfg {
  static constexpr int NP = SSM ? 18 * NCH : 108, NP1 = NP / 2, NM = 4 * NP, NM1 = NM / 2, NI = NP;
  static constexpr int PITCH = SSM ? 8448 : 8192, NSLOT = SSM ? 18 : 19;
  static constexpr int UNIT2 = SSM ? 4 * NCH : 24;
  static constexpr int MISC = NSLOT * PITCH;
};
constexpr bool bar_at(int m) { return m % 12 == 0 || (m < 24 && m % 4 == 0); }
constexpr int next_bar(int m) { int b = m + 1; while (!bar_at(b)) ++b; return b; }
constexpr int pslot(int ch) { return ch == 0 ? 17 : ch == 1 ? 15 : ch == 2 ? 13 : ch == 3 ? 11 : ch == 4 ? 17 : 15; }
constexpr int HSLOT = 8, KSLOT = 14;
template <bool SSM> constexpr int p2_dp(int j) { return SSM ? j / NCH : (j < 12 ? (j & 1) : j / 6); }
template <bool SSM> constexpr int p2_ch(int j) { return SSM ? j % NCH : (j < 12 ? (j >> 1) : j % 6); }
constexpr int h_ch(int n) { return n >= 16 ? 4 : 2 * (n >> 3) + ((n >> 1) & 1); }
constexpr int h_kk(int n) { return n >= 16 ? n & 3 : 2 * ((n >> 2) & 1) + (n & 1); }
template <bool SSM> constexpr int store_of(int m) {
  using C = Cfg<SSM>; const int m2 = m - C::NM1;
  if (m2 < 0) return -1;
  if (!SSM && m2 < 72) return m2 == 50 ? 0 : m2 == 54 ? 1 : m2 == 58 ? 2 : m2 == 62 ? 3 : -1;
  if (m2 < C::UNIT2) return -1;
  return m2 % C::UNIT2 == 4 ? 2 * (m2 / C::UNIT2 - 1) : m2 % C::UNIT2 == 8 ? 2 * (m2 / C::UNIT2 - 1) + 1 : -1;
}
constexpr int pf_of(int m) { return (m >= 74 && m < 74 + 8 * 9 && (m - 74) % 8 == 0) ? (m - 74) / 8 : -1; }
constexpr int NXP = 6;
#define I0 (C::NSLOT - 2 * NXP)
constexpr int xslot(int p) { return p < NXP ? 2 * p : 2 * p - 8; }
constexpr int JORD[18] = {0, 2, 4, 6, 1, 3, 5, 7, 8, 9, 10, 11, 12, 13, 14, 15, 16, 17};
constexpr int JC[10] = {0, 0, 0, 2, 2, 3, 0, 0, 0, 0}, N0[10] = {0, 0, 0, 0, 0, 4, 0, 0, 0, 0}, N1[10] = {0, 0, 0, 0, 0, 3, 0, 0, 0, 0}, FD[10] = {0, 0, 0, 0, 0, 10, 0, 0, 0, 0};
constexpr int JE0 = 5, MAXPUB = 6;
constexpr int jcount(int e) { return e >= 0 && e < 10 ? JC[e] : 0; }
constexpr int jfirst(int e) { int n = 0; for (int k = 9; k > e; --k) n += JC[k]; return n; }
constexpr int NJIN = jfirst(0);
template <bool SSM> constexpr int last_store(int dp) { for (int m = Cfg<SSM>::NM - 1; m >= 0; --m) if (store_of<SSM>(m) == 2 * dp + 1) return m; return 100000; }
template <bool SSM> constexpr int pub_count(int B) { int n = 0;
  while (n < MAXPUB && last_store<SSM>(n) + (last_store<SSM>(n) < Cfg<SSM>::NM - 12 * JE0 ? 14 : SSM ? 20 : 26) <= B) ++n; return n; }
template <bool SSM> constexpr bool pub_at(int B) { return B % 12 == 0 && (pub_count<SSM>(B) == (SSM ? 3 : 4) || pub_count<SSM>(B) == MAXPUB) && pub_count<SSM>(B) != pub_count<SSM>(B - 12); }
template <bool SSM> constexpr int chk_at(int B) { const int e = (Cfg<SSM>::NM - B) / 12; return (B % 12 == 0 && e > 0 && e < 10 && FD[e]) ? e : 0; }
template <bool SSM> constexpr int fld_of(int B) { return B - FD[chk_at<SSM>(B)]; }
template <bool SSM> constexpr bool fld_at(int m) { for (int e = 1; e < 10; ++e) if (FD[e] && Cfg<SSM>::NM - 12 * e - FD[e] == m) return true; return false; }
template <bool SSM> constexpr int job_at(int m) {
  const int e = (Cfg<SSM>::NM - (m - m % 12)) / 12, k = (m % 12) / 4; return (m % 4 == 2 && k < jcount(e)) ? jfirst(e) + k : -1; }
constexpr int XS = 26, XP = 84;
constexpr int xld_at(int m) { return (m >= XS && m < XS + 9) ? m - XS : -1; }
struct Sched { int slot[126]; int istep[126]; int vm[432]; int nis[432]; int fis[432]; int jslot[18]; bool ok; };
template <bool SSM> constexpr Sched make_sched() {
  using C = Cfg<SSM>;
  Sched s{};
  int free_at[19] = {}, b0[19] = {}, b1[19] = {};
  for (int i = 0; i < 19; ++i) { free_at[i] = i < 2 * NXP ? ((i >= 2 * NXP - 8 && i < 10) ? 20 + 4 * (i >> 1) : 4 + 4 * (i >> 1)) : -1; b0[i] = 1000; b1[i] = 1000; }
  if (SSM) { for (int i = HSLOT; i < 18; ++i) { b0[i] = i >= KSLOT ? 72 : C::NM1; b1[i] = C::NM1 + (i < HSLOT + 7 ? 0 : 12); } }
  else { b0[17] = b0[18] = 72; b1[17] = b1[18] = 240; b0[15] = b0[16] = 108; b1[15] = b1[16] = 264; b0[13] = b0[14] = 144; b1[13] = b1[14] = 204; b0[11] = b0[12] = 180; b1[11] = b1[12] = 216; }
  int q = 0; bool jok = true;
  for (int B = -1; B < C::NM; B = B < 0 ? 0 : next_bar(B)) {
    const int len = B < 0 ? I0 : next_bar(B) - B, maxi = B < 0 ? I0 : len == 4 ? 1 + ((B >> 2) & 1) : 5;
    if (B >= 36) for (int k = 0; k < jcount((C::NM - B) / 12); ++k) {
      int best = -1; for (int i = 0; i < C::NSLOT; ++i) if (free_at[i] <= B) { best = i; break; }
      if (best < 0) jok = false; else { s.jslot[JORD[jfirst((C::NM - B) / 12) + k]] = best; free_at[best] = 100000; } }
    int n = 0;
    while (n < maxi && q + n < C::NI && q + n < (B < 0 ? 0 : B) / 4 + 18) {
      const int it = q + n, end = next_bar(4 * it + 3); int best = -1;
      int key = 100000;
      for (int i = C::NSLOT - 1; i >= 0; --i) if (free_at[i] <= B && (end <= b0[i] || B >= b1[i]) && !(SSM && it >= C::NP1 - 14 && it < C::NP1 && (i >= HSLOT) != (it >= C::NP1 - 6))) { const int k2 = B >= b1[i] ? 1000 : b0[i]; if (k2 < key) { key = k2; best = i; } }
      if (best < 0) break;
      s.slot[it] = best; free_at[best] = end; ++n;
    }
    for (int k = 0; k < n; ++k) s.istep[q + k] = B < 0 ? -1 : len == 4 ? B + 2 * k + 1 : B + (n <= 3 ? 4 * k + 3 : n == 4 ? 3 * k + 1 : 2 * k + 1);
    q += n;
  }
  for (int k = NJIN; k < 18; ++k) { int best = -1; for (int i = 0; i < C::NSLOT; ++i) if (free_at[i] <= C::NM) { best = i; break; }
    if (best < 0) jok = false; else { s.jslot[JORD[k]] = best; free_at[best] = 100000; } }
  s.ok = q == C::NI && jok;
  for (int m = 0; m < C::NM; ++m) { s.nis[m] = 0; s.fis[m] = 0; }
  for (int it = C::NI - 1; it >= 0; --it) if (s.istep[it] >= 0) { s.nis[s.istep[it]]++; s.fis[s.istep[it]] = it; }
  for (int B = 0; B < C::NM; B = next_bar(B)) {
    int np = next_bar(B) / 4; if (np > C::NP - 1) np = C::NP - 1;
    const int need = np;
    if (s.istep[need] >= B) { s.ok = false; continue; }
    int cnt = 0;
    for (int it = need + 1; it < C::NI; ++it) if (s.istep[it] < B) ++cnt;
    for (int m = s.istep[need] + 1; m < B; ++m) if (m >= 0) cnt += (store_of<SSM>(m) >= 0) + (pf_of(m) >= 0) + (xld_at(m) >= 0) + fld_at<SSM>(m) + (job_at<SSM>(m) >= 0);
    for (int t = 0; t < 3; ++t) {
      const int m0 = t == 0 ? (pub_at<SSM>(B) ? last_store<SSM>(pub_count<SSM>(B) - 1) : -1) : t == 1 ? (chk_at<SSM>(B) ? fld_of<SSM>(B) : -1) : (B == XP ? XS + 8 : -1);
      if (m0 < 0) continue;
      int c2 = (t == 0 ? fld_at<SSM>(m0) : 0) + (job_at<SSM>(m0) >= 0) + s.nis[m0];
      for (int m = m0 + 1; m < B; ++m) c2 += (store_of<SSM>(m) >= 0) + fld_at<SSM>(m) + (job_at<SSM>(m) >= 0) + s.nis[m];
      if (c2 < cnt) cnt = c2;
    }
    s.vm[B] = cnt; if (cnt > 63) s.ok = false;
  }
  return s;
}
template <bool SSM> struct SC { static constexpr Sched s = make_sched<SSM>(); };
static_assert(SC<false>::s.ok && SC<true>::s.ok, "bad schedule");

struct Args {
  const u16 *x, *lre, *lim, *bre, *bim, *cre, *cim, *ldt, *dsk, *w1, *b1, *w2, *b2;
  char* ws; float* out;
};

typedef const __attribute__((address_space(4))) Args* KArgs;
DI KArgs kargs() { KArgs p = (KArgs)__builtin_amdgcn_kernarg_segment_ptr(); asm volatile("" : "+s"(p)); return p; }
template <bool SSM> DI void body(const unsigned lds0, const int slice, const int rb, const char* code) {
  using C = Cfg<SSM>;
#define S SC<SSM>::s
  Args a;
  { KArgs k = kargs(); a.x = k->x; a.ws = k->ws;
    if constexpr (SSM) { a.lre = k->lre; a.lim = k->lim; a.ldt = k->ldt; a.bre = k->bre; a.bim = k->bim; a.cre = k->cre; a.cim = k->cim; }
    else { a.w1 = k->w1; a.b1 = k->b1; a.w2 = k->w2; }
    a.b2 = k->b2; a.dsk = k->dsk;
    if constexpr (SSM) asm volatile("" : "+s"(a.lre), "+s"(a.lim), "+s"(a.ldt), "+s"(a.b2), "+s"(a.dsk)); else asm volatile("" : "+s"(a.b1), "+s"(a.b2), "+s"(a.dsk)); }
  constexpr int PITCH = C::PITCH, TAB = C::MISC, TAB1 = TAB + NSIG * 16, SEG = TAB1 + NSIG * 16, FST = LDS_BYTES - 512, ETAB = FST - 576;
  static_assert(!SSM || SEG + 4 * NSIG * 8 <= ETAB, "");
  int tid = threadIdx.x, lane = tid & 63;
  const int w = __builtin_amdgcn_readfirstlane(tid >> 6), r = w & 3, c = w >> 2;
  const int f0 = slice * FS, t0 = rb * 128;
  const int sg = slice - NFFN, p0 = 128 * sg, p4 = 512 + 32 * (sg & 1);
  int h = lane >> 5, l31 = lane & 31;
  const int drow = 8 * w + (lane >> 3), dcp = lane & 7;
  const unsigned voffX = (lane >> 3) * 1152 + ((dcp ^ ((drow >> 1) & 7)) << 4);
  const int xrot = 4 * (slice >> 2);
  const int xblk[2] = {(w + xrot) & 15, (w + 8 + xrot) & 15};
  const char* xb = (const char*)(a.x + (size_t)t0 * D);
  const unsigned ddst = lds0 + w * (SSM ? 1056 : 1024);
  unsigned voff1, voff2; const char *sb1, *sb2;
  if constexpr (SSM) {
    const int sw = dcp ^ ((drow >> 1) & 1);
    voff1 = (16 * c + 8 * (w & 1) + (lane >> 3)) * 1152 + (sw << 4);
    sb1 = (const char*)(((w >> 1) & 1) ? a.bim : a.bre);
    sb2 = (const char*)(c ? a.cim : a.cre);
  } else {
    const unsigned sw = (dcp ^ (((drow >> 1) & 1) << 2)) << 4;
    voff1 = drow * 9216 + sw; voff2 = drow * 1152 + sw;
    sb1 = (const char*)(a.w1 + f0); sb2 = (const char*)(a.w2 + (size_t)f0 * D);
  }
  auto issue = [&](auto it_) {
    constexpr int p = it_, q = p - C::NP1, so = S.slot[p] * PITCH;
    if constexpr (SSM) {
      if constexpr (p < C::NP1) dma1(voff1, sb1 + (p / 9 < 4 ? p0 + (p / 9) * 32 : p4) * 1152 + (p % 9) * 128, ddst + so);
      else {
        if constexpr (q == 0) { int tz = threadIdx.x; PIN(tz); const int dr = (tz >> 2) & 63, d8 = tz >> 3;
          voff2 = dr * 1152 + (((tz & 3) ^ ((dr >> 3) & 1)) << 4); voff1 = d8 * 1152 + (((tz & 7) ^ ((d8 >> 1) & 1)) << 4); }
        constexpr int dp = q / NCH, j = q % NCH;
        if constexpr (j < 4) dma1(voff1, (const char*)((j & 1) ? a.cim : a.cre) + dp * (64 * 1152) + p0 * 2 + (j >> 1) * 128, ddst + so);
        else dma1(voff2, sb2 + dp * (64 * 1152) + p4 * 2, ddst + so);
      }
    } else {
      if constexpr (p < C::NP1) dma1(voff1, sb1 + (p / 9) * 128 + (size_t)(p % 9) * (64 * 9216), ddst + so);
      else dma1(voff2, sb2 + p2_dp<SSM>(q) * 128 + p2_ch<SSM>(q) * (64 * 1152), ddst + so);
    }
  };
  const int jd0 = (slice >> 3) * 32 + (slice & 1) * 16, jt0 = ((slice >> 1) & 3) * 32;
  unsigned rtag = *(const __attribute__((address_space(4))) unsigned*)(a.ws + WS_REPOCH + rb * 256) + 1u;
  unsigned tag = 0;
  if constexpr (SSM) tag = *(const __attribute__((address_space(4))) unsigned*)(a.ws + WS_EPOCH + sg * 256) + 1u;
  u32x2 braw; unsigned plr, pli, pdt, eb2, edk;
  if (w < 3) {
    if constexpr (SSM) {
      const int sp = tid < 128 ? p0 + tid : p4 + (tid & 31);
      asm volatile("global_load_ushort %0, %1, off" : "=v"(plr) : "v"(a.lre + sp) : "memory");
      asm volatile("global_load_ushort %0, %1, off" : "=v"(pli) : "v"(a.lim + sp) : "memory");
      asm volatile("global_load_ushort %0, %1, off" : "=v"(pdt) : "v"(a.ldt + sp) : "memory");
    } else {
      asm volatile("global_load_dwordx2 %0, %1, off" : "=v"(braw) : "v"(a.b1 + f0 + 4 * (tid < 96 ? tid : 95)) : "memory");
    }
    const int ti = min(tid, 143), d = (ti >> 4) * 64 + jd0 + (ti & 15);
    asm volatile("global_load_ushort %0, %1, off" : "=v"(eb2) : "v"(a.b2 + d) : "memory");
    asm volatile("global_load_ushort %0, %1, off" : "=v"(edk) : "v"(a.dsk + d) : "memory");
  } else if (w == 3) {
    touch(min(lane, 14) * 128, code, lds0 + LDS_BYTES - 256);
  }
  u32x4 xr[18];
  sfor<0, 9>([&](auto k_) { gload16<k_ * 128>(xr[k_], voffX, xb + xblk[0] * 9216); });
  u32x4 xf[36];
  unsigned xa[2][4];
  {
    const int xrow = 32 * (r & 1) + (SSM ? 16 * ((l31 >> 2) & 1) + (l31 & 3) + 4 * (l31 >> 3) : l31), xkey = (xrow >> 1) & 7;
#pragma unroll
    for (int kk = 0; kk < 4; ++kk) { xa[0][kk] = lds0 + (r >> 1) * PITCH + xrow * 128 + (((2 * kk + h) ^ xkey) << 4); xa[1][kk] = xa[0][kk] + 8 * PITCH; PIN(xa[0][kk]); PIN(xa[1][kk]); } }
  unsigned fb1[3], fb2[3];
  if constexpr (SSM) {
    const int row = 32 * c + l31;
#pragma unroll
    for (int i = 0; i < 3; ++i) fb1[i] = lds0 + (row >> 3) * 1056 + (row & 7) * 128 + ((h ^ ((row >> 1) & 1)) << 4) + i * 65536;
  } else {
    const int q4 = (lane & 15) >> 2, pq = lane & 3, cb = (lane >> 4) & 1;
    const unsigned colb = (((c * 4 + cb * 2 + (pq >> 1)) ^ ((q4 >> 1) << 2)) << 4) + 8 * (pq & 1);
#pragma unroll
    for (int i = 0; i < 3; ++i) { fb1[i] = lds0 + (8 * h + q4) * 128 + colb + i * 65536; fb2[i] = lds0 + (4 * h + q4) * 128 + colb + i * 65536; }
  }
  PIN(fb1[0]); PIN(fb1[1]); PIN(fb1[2]); PIN(voff1);
  auto fread = [&](auto m_) -> bf16x8 {
    constexpr int p = m_ / 4, kk = m_ & 3;
    constexpr bool mixed = SSM && p >= C::NP1 && (p - C::NP1) % NCH == 4;
    constexpr int off = S.slot[p] * PITCH + (!SSM ? kk * 2048 : !mixed ? kk * 32 : (kk >> 1) * 4224 + (kk & 1) * 32);
    if constexpr (SSM && m_ == C::NM1 + 16) { int tz = threadIdx.x; PIN(tz); const int row = 32 * c + (tz & 31);
#pragma unroll
      for (int i = 0; i < 3; ++i) { fb2[i] = lds0 + (row >> 4) * 1056 + (row & 15) * 64 + ((((tz >> 5) ^ (row >> 3)) & 1) << 4) + i * 65536; pin1(fb2[i]); } }
    if constexpr (mixed) return __builtin_bit_cast(bf16x8, lds128(fb2[off >> 16] + (off & 65535)));
    else if constexpr (SSM) return __builtin_bit_cast(bf16x8, lds128(fb1[off >> 16] + (off & 65535)));
    else if constexpr (p < C::NP1) return cat(trd(fb1[off >> 16] + (off & 65535)), trd(fb1[off >> 16] + (off & 65535) + 512));
    else return cat(trd(fb2[off >> 16] + (off & 65535)), trd(fb2[off >> 16] + (off & 65535) + 1024)); };
  auto xread = [&](int m) { const int sl = xslot(m >> 2); xf[m] = lds128(xa[sl >> 3][m & 3] + (sl & 7) * PITCH); };
  unsigned pwr, prd, biasb;
  if constexpr (!SSM) { pwr = lds0 + w * 2048 + lane * 16 + 11 * PITCH; prd = lds0 + r * 2048 + lane * 16 + 11 * PITCH; PIN(pwr);
    biasb = lds0 + TAB + (32 * c + 4 * h) * 4; PIN(biasb); }
  unsigned xw[2][2];
#pragma unroll
  for (int i = 0; i < 2; ++i) { xw[i][0] = lds0 + (xblk[i] & 7) * 1024 + (xblk[i] >> 3) * PITCH + lane * 16; xw[i][1] = xw[i][0] + 8 * PITCH; PIN(xw[i][0]); PIN(xw[i][1]); }
  auto xput = [&](int k) { const int sl = xslot(k % 9); *(LAS u32x4*)(xw[k / 9][sl / 8] + (sl % 8) * PITCH) = xr[k]; };
  u32x2 ones = {0x3f803f80u, 0x3f803f80u}; PIN(ones);
  sfor<0, 2>([&](auto hf_) { constexpr int hf = hf_;
    f32x4 sa[2] = {}, sq[2] = {};
    sfor<0, 3>([&](auto g_) { constexpr int k0 = 9 * hf + 3 * g_;
      wait3<(hf ? 15 - k0 + I0 : 6)>(xr + k0);
      if constexpr (hf == 0) sfor<k0, k0 + 3>([&](auto k_) { gload16<k_ * 128>(xr[9 + k_], voffX, xb + xblk[1] * 9216); });
#pragma unroll
      for (int k = k0; k < k0 + 3; ++k)
#pragma unroll
        for (int e = 0; e < 2; ++e) { const u32x2 v = {xr[k][2 * e], xr[k][2 * e + 1]}; sa[e] = mf4(ones, v, sa[e]); sq[e] = mf4(v, v, sq[e]); } });
    const f32x4 q4 = sq[0] + sq[1];
    float d0 = q4[0], d1 = q4[1], d2 = q4[2], d3 = q4[3]; PIN(d1); PIN(d3);
    d0 = (lane & 1) ? d1 : d0; d2 = (lane & 1) ? d3 : d2;
    float s1 = sa[0][0] + sa[1][0], s2 = (lane & 2) ? d2 : d0;
    s1 += dpp<0xB1>(s1); s2 += dpp<0xB1>(s2); s1 += dpp<0x4E>(s1); s2 += dpp<0x4E>(s2); s1 += dpp<0x141>(s1); s2 += dpp<0x141>(s2);
    const float mean = s1 * (1.f / D);
    const float var = fmaxf(s2 * (1.f / D) - mean * mean, 0.f);
    const float mb = rbf(mean), iden = __builtin_amdgcn_rcpf(rbf(__builtin_amdgcn_sqrtf(rbf(rbf(var) + 1e-5f))));
    const float inv = (SSM ? 1.f : BETA) * iden, ih = rbf(inv), il = rbf(inv - ih), nb = -mb * (ih + il);
    if ((xblk[hf] >> 2) == (jt0 >> 5)) *(LAS f32x2*)(lds0 + FST + ((xblk[hf] & 3) * 8 + (lane >> 3)) * 8) = (f32x2){mb, iden};
    const unsigned bh = __float_as_uint(ih) >> ((lane & 1) ? 0 : 16), bl = __float_as_uint(il) >> ((lane & 1) ? 0 : 16);
    const u32x2 Ah = {(lane & 2) ? 0u : bh, (lane & 2) ? bh : 0u}, Al = {(lane & 2) ? 0u : bl, (lane & 2) ? bl : 0u};
    const f32x4 nv = {nb, nb, nb, nb};
#pragma unroll
    for (int k3 = 9 * hf; k3 < 9 * hf + 9; k3 += 3) {
      f32x4 t[6];
#pragma unroll
      for (int e = 0; e < 6; ++e) t[e] = mf4(Al, (u32x2){xr[k3 + (e >> 1)][2 * (e & 1)], xr[k3 + (e >> 1)][2 * (e & 1) + 1]}, nv);
#pragma unroll
      for (int e = 0; e < 6; ++e) t[e] = mf4(Ah, (u32x2){xr[k3 + (e >> 1)][2 * (e & 1)], xr[k3 + (e >> 1)][2 * (e & 1) + 1]}, t[e]);
#pragma unroll
      for (int e = 0; e < 6; ++e) { xr[k3 + (e >> 1)][2 * (e & 1)] = pk2(t[e][0], t[e][1]); xr[k3 + (e >> 1)][2 * (e & 1) + 1] = pk2(t[e][2], t[e][3]); }
#pragma unroll
      for (int k = k3; k < k3 + 3; ++k) if (k % 9 < NXP) xput(k);
    }
    if constexpr (hf == 0) sfor<0, I0>(issue);
  });
  if (w < 3) {
    PIN(eb2); PIN(edk);
    *(LAS unsigned*)(lds0 + ETAB + min(tid, 143) * 4) = eb2 | (edk << 16);
    if constexpr (SSM) {
      PIN(plr); PIN(pli); PIN(pdt);
      if (tid < NSIG) {
        const float lr = lo2f(plr), li = lo2f(pli), dt = rbf(expf(lo2f(pdt)));
        const float e = expf(dt * lr); float sn, cs; sincosf(dt * li, &sn, &cs);
        const float ar = e * cs, ai = e * sn, nr = ar - 1.f, den = lr * lr + li * li;
        const float k2 = (sg >= 2 && tid >= 128) ? 0.f : 2.f / den;
        *(LAS f32x4*)(lds0 + TAB + tid * 16) = (f32x4){ar, ai, (nr * lr + ai * li) * k2, (ai * lr - nr * li) * k2};
        const cf a8 = csq(csq(csq(cf{ar, ai}))), a16 = csq(a8);
        *(LAS f32x4*)(lds0 + TAB1 + tid * 16) = (f32x4){a8.r, a8.i, a16.r, a16.i};
      }
    } else {
      PIN(braw);
      if (tid < 96) *(LAS f32x4*)(lds0 + TAB + tid * 16) = (f32x4){BETA * lo2f(braw[0]), BETA * hi2f(braw[0]), BETA * lo2f(braw[1]), BETA * hi2f(braw[1])};
    }
  }
  asm volatile("" : "+s"(rtag), "+s"(tag));
  wait_bar<S.vm[0], 0>();
  constexpr int PF = 3, NB = PF + 1;
  bf16x8 af[NB];
  f32x16 acc, prev, oacc[9];
  u32x4 P[6][4], pn[2], nbias[4];
  af[0] = fread(ic<0>{}); xread(0);
  if constexpr (!SSM) {
#pragma unroll
    for (int g = 0; g < 4; ++g) nbias[g] = lds128(biasb + g * 32); }
  af[1] = fread(ic<1>{}); xread(1); af[2] = fread(ic<2>{}); xread(2);
  cf u[NCH][8];
  const f32x16 zero16 = {};
  float gt0, gt1;
  char* pbase; unsigned pvo, jvo, xo; float xp[5], xt[9];
  auto gelu_ph = [&](int e, int ph) {
    const float v0 = prev[2 * e], v1 = prev[2 * e + 1];
    if (ph == 0) { gt0 = v0 * __builtin_fmaf(v0, v0, KAP); gt1 = v1 * __builtin_fmaf(v1, v1, KAP); PIN(gt0); PIN(gt1); }
    if (ph == 1) { gt0 = __builtin_amdgcn_exp2f(-gt0); gt1 = __builtin_amdgcn_exp2f(-gt1); PIN(gt0); PIN(gt1); }
    if (ph == 2) { gt0 = __builtin_amdgcn_rcpf(1.f + gt0); gt1 = 1.f + gt1; PIN(gt0); PIN(gt1); }
    if (ph == 3) { unsigned pv = pk2(v0 * gt0, v1 * __builtin_amdgcn_rcpf(gt1)); PIN(pv); pn[e >> 2][e & 3] = pv; }
  };
  cf sE, sH, sU;
  f32x4 stab, stab1;
  unsigned segw;
#define PINC(z) do { if (pin) { PIN((z).r); PIN((z).i); } } while (0)
  auto pass1 = [&](int ch, int k, bool pin) {
    if (k == 0) { int tz = threadIdx.x; PIN(tz); segw = lds0 + (32 * ch + 16 * c + (tz & 15)) * 8;
      stab = *(LAS f32x4*)(2 * segw - lds0 + TAB); }
    const cf av = {stab[0], stab[1]};
    if (k >= 1 && k <= 4) { for (int j = 2 * k - 2; j < 2 * k; ++j) { swap16(prev[j], prev[j + 8], u[ch][j].r, u[ch][j].i); PINC(u[ch][j]); } }
    if (k >= 5 && k <= 11) { const int i = k - 5; sE = cmad(av, i ? sE : u[ch][0], u[ch][i + 1]); PINC(sE); }
    if (k == 9) stab1 = *(LAS f32x4*)(2 * segw - lds0 + TAB1);
    const cf a8 = {stab1[0], stab1[1]}, a16 = {stab1[2], stab1[3]};
    if (k == 12) { swap16(sE.r, sE.r, sE.r, sH.r); swap16(sE.i, sE.i, sE.i, sH.i); PINC(sH); PINC(sE); }
    if (k == 13) { sU = cmad(a8, sE, sH); PINC(sU); }
    if (k == 14) { swap32(sU.r, sU.r, sH.r); swap32(sU.i, sU.i, sH.i); PINC(sH); PINC(sU); }
    if (k == 15) { cf Sg = cmad(a16, sU, sH); PINC(Sg); *(LAS f32x2*)(segw + SEG + r * NSIG * 8) = (f32x2){Sg.r, Sg.i}; }
    if (k == 16) { sU = {selm(0.f, sU.r, HMASK), selm(0.f, sU.i, HMASK)}; sE = cmad(a8, sU, sE); PINC(sU); PINC(sE); }
    if (k == 17) { u[ch][0] = cmad(av, cf{selm(sU.r, sE.r, GMASK), selm(sU.i, sE.i, GMASK)}, u[ch][0]); PINC(u[ch][0]); }
    if (k >= 18) { const int j = k - 18; u[ch][j] = cmul(cf{stab[2], stab[3]}, u[ch][j]); PINC(u[ch][j]); }
  };
  char* const granb = a.ws + WS_GRAN + (size_t)(sg * 16 + 15 - rb) * (NSIG * 16);
  auto seg_rd = [&](int t, cf* sv) {
#pragma unroll
    for (int i = 0; i < 4; ++i) { const f32x2 v = *(LAS f32x2*)(lds0 + SEG + (i * NSIG + t) * 8); sv[i] = {v[0], v[1]}; } };
  auto pow32 = [&](int t) { const f32x2 v = *(LAS f32x2*)(lds0 + TAB1 + t * 16 + 8); return csq(cf{v[0], v[1]}); };
  auto carry_out = [&](int t, cf a32) { cf sv[4]; seg_rd(t, sv);
    const cf T = cmad(a32, cmad(a32, cmad(a32, sv[0], sv[1]), sv[2]), sv[3]);
    __builtin_amdgcn_raw_buffer_store_b128((u32x4){__float_as_uint(T.r), tag, __float_as_uint(T.i), tag}, __builtin_amdgcn_make_buffer_rsrc(granb, 0, NSIG * 16, 0x00020000), t * 16, 0, 16); };
  auto carry_ld = [&](int t, u32x4* v) {
    const auto rs = __builtin_amdgcn_make_buffer_rsrc(granb + NSIG * 16, 0, rb * (NSIG * 16), 0x00020000);
    unsigned vo = t * 16; PIN(vo);
#pragma unroll
    for (int k = 0; k < 15; ++k) v[k] = __builtin_amdgcn_raw_buffer_load_b128(rs, vo + k * (NSIG * 16), 0, 16); };
  auto carry_in = [&](int t, cf a32, u32x4* v) {
    cf sv[4]; seg_rd(t, sv);
    const cf a128 = csq(csq(a32)), a512 = csq(csq(a128));
#pragma unroll 1
    for (unsigned spins = 0; spins < (1u << 16); ++spins) {
      unsigned sum = 0;
#pragma unroll
      for (int k = 0; k < 15; ++k) sum += v[k][1] + v[k][3];
      if (__all(sum == 2u * rb * tag)) break;
      carry_ld(t, v);
    }
    cf q[4];
#pragma unroll
    for (int i = 3; i >= 0; --i)
#pragma unroll
      for (int n = 0; n < 4; ++n) { const int k = 4 * n + i; if (k > 14) continue;
        const cf x = {__uint_as_float(v[k][0]), __uint_as_float(v[k][2])}; q[n] = (i == 3 || k == 14) ? x : cmad(a128, q[n], x); }
    cf G = cmad(a512, cmad(a512, cmad(a512, q[3], q[2]), q[1]), q[0]);
#pragma unroll
    for (int i = 0; i < 4; ++i) { *(LAS f32x2*)(lds0 + SEG + (i * NSIG + t) * 8) = (f32x2){G.r, G.i}; G = cmad(a32, G, sv[i]); } };
  unsigned hr[2][2];
  auto hread = [&](int n) { const int ch = h_ch(n), kk = h_kk(n); P[ch][kk] = __builtin_bit_cast(u32x4, cat(trd(hr[ch >> 2][0] + (ch & 3) * 16384 + kk * 1024), trd(hr[ch >> 2][1] + (ch & 3) * 16384 + kk * 1024))); };
  auto ssm_boundary = [&]() {
    tid = threadIdx.x; PIN(tid); lane = tid & 63; h = lane >> 5;
    const int ts = min(tid, NSIG - 1);
    u32x4 v[15];
    if (w < 2) carry_ld(ts, v);
    const cf a32 = pow32(ts);
#pragma unroll
    for (int k = 0; k < 16; ++k) pass1(NCH - 1, k, false);
    const int g = (lane >> 4) & 1, s16 = lane & 15;
#pragma unroll
    for (int k = 0; k < 4; ++k) { const f32x4 t = *(LAS f32x4*)(lds0 + KSLOT * PITCH + tid * 16 + k * 8192); u[0][2 * k] = {t[0], t[1]}; u[0][2 * k + 1] = {t[2], t[3]}; }
    SBAR();
    if (w < 2) carry_in(ts, a32, v);
    wait_bar<63, 0>();
    if (w == 2) carry_out(ts, a32);
    const int q4 = (lane & 15) >> 2, pq = lane & 3;
    unsigned hw[2];
    hw[0] = lds0 + HSLOT * PITCH + r * 4096 + (16 * c + s16) * 64 + (((2 * h + g) ^ ((s16 >> 1) & 3)) << 4); hw[1] = hw[0] + 65536;
    PIN(hw[0]); PIN(hw[1]);
#pragma unroll
    for (int k = 0; k < 2; ++k) {
      hr[0][k] = lds0 + HSLOT * PITCH + r * 4096 + (8 * h + 4 * k + q4) * 64 + (((2 * g + (pq >> 1)) ^ (2 * k + (q4 >> 1))) << 4) + (pq & 1) * 8; hr[1][k] = hr[0][k] + 65536;
      PIN(hr[0][k]); PIN(hr[1][k]); }
    cf av[NCH], pw[NCH];
    auto prep = [&](int ch) {
      const f32x4 t = *(LAS f32x4*)(lds0 + TAB + (32 * ch + 16 * c + s16) * 16), t1 = *(LAS f32x4*)(lds0 + TAB1 + (32 * ch + 16 * c + s16) * 16);
      av[ch] = {t[0], t[1]};
      pw[ch] = cmul(cmul(cf{t[2], t[3]}, cf{selm(1.f, t1[0], GMASK), selm(0.f, t1[1], GMASK)}), cf{selm(1.f, t1[2], HMASK), selm(0.f, t1[3], HMASK)}); };
    auto fin = [&](int c0, int c1) {
      f32x2 z[NCH], o[NCH][8];
#pragma unroll
      for (int ch = c0; ch < c1; ++ch) { const f32x2 ci = *(LAS f32x2*)(lds0 + SEG + (r * NSIG + 32 * ch + 16 * c + s16) * 8);
        const cf z0 = cmul(pw[ch], cf{ci[0], ci[1]}); z[ch] = (f32x2){z0.r, z0.i}; }
#pragma unroll
      for (int i = 0; i < 8; ++i)
#pragma unroll
        for (int ch = c0; ch < c1; ++ch) {
          const f32x2 t = __builtin_elementwise_fma((f32x2){av[ch].r, av[ch].r}, z[ch], (f32x2){u[ch][i].r, u[ch][i].i});
          z[ch] = __builtin_elementwise_fma((f32x2){-av[ch].i, av[ch].i}, __builtin_shufflevector(z[ch], z[ch], 1, 0), t); o[ch][i] = z[ch]; }
#pragma unroll
      for (int ch = c0; ch < c1; ++ch) {
        *(LAS u32x4*)(hw[ch >> 2] + (ch & 3) * 16384) = (u32x4){pk2(o[ch][0][0], o[ch][1][0]), pk2(o[ch][2][0], o[ch][3][0]), pk2(o[ch][4][0], o[ch][5][0]), pk2(o[ch][6][0], o[ch][7][0])};
        *(LAS u32x4*)(hw[ch >> 2] + (ch & 3) * 16384 + 2048) = (u32x4){pk2(-o[ch][0][1], -o[ch][1][1]), pk2(-o[ch][2][1], -o[ch][3][1]), pk2(-o[ch][4][1], -o[ch][5][1]), pk2(-o[ch][6][1], -o[ch][7][1])};
      } };
#pragma unroll
    for (int ch = 0; ch < NCH; ++ch) prep(ch);
    fin(0, 4);
    SBAR();
    if (w == 2) carry_ld(ts, v);
#pragma unroll
    for (int k = 16; k < 26; ++k) pass1(NCH - 1, k, false);
    SBAR();
    if (w == 2) carry_in(ts, a32, v);
    wait_bar<63, 0>();
    if (rb == 15 && tid == 0) __hip_atomic_store((gu32*)(a.ws + WS_EPOCH + sg * 256), tag, RLX_AGENT);
#pragma unroll
    for (int n = 0; n < 16; ++n) hread(n);
    fin(4, 5);
  };
  unsigned pfo;
  { const int ln = min(lane, 2); pfo = SSM ? (ln < 2 ? p0 * 2 + ln * 128 : 1024) : ln * 128; PIN(pfo); }
  char* const flagb = a.ws + WS_FLAG + rb * 4096;
  u32x2 fl;
  auto publish = [&](unsigned n) { flag_store(flagb + slice * 256, n, rtag, w); };
  auto await = [&](u32x2 v, unsigned n0, unsigned n1) {
    const unsigned need = (pvo & 512) ? n1 : n0;
    if (__builtin_expect(!__all(need == 0 || (v[1] == rtag && v[0] >= need)), 0)) {
#pragma unroll 1
      for (unsigned spins = 0; spins < (1u << 16); ++spins) {
        const unsigned long long t = __hip_atomic_load((gu64*)(flagb + w * 256 + (pvo & 512) * 4), RLX_AGENT);
        if (__all(need == 0 || ((unsigned)(t >> 32) == rtag && (unsigned)t >= need))) break;
        __builtin_amdgcn_s_sleep(2);
      }
    } };
  const char* const jsrc = a.ws + WS_PART + (size_t)(w * 16 + rb) * PART_BLK + slice * 1024;
  auto jissue = [&](auto j_) { constexpr int j = j_, so = S.jslot[j] * PITCH;
    dma1s(jvo, jsrc + (j >> 1) * 16384 + (size_t)(j & 1) * (128 * PART_BLK), lds0 + w * 1024 + so); };
  sfor<0, C::NM / 4>([&](auto p_) { sfor<4 * p_, 4 * p_ + 4>([&](auto m_) {
    constexpr int m = m_;
    constexpr bool ph1 = m < C::NM1;
    constexpr int un = m / 36, gn = m % 36, m2 = m - C::NM1;
    if constexpr (m == C::NM1) {
      if constexpr (SSM) ssm_boundary();
      int tz = threadIdx.x; PIN(tz);
      pvo = (tz & 63) * 16; jvo = pvo ^ (r << 5) ^ ((tz & 32) << 2); pbase = a.ws + WS_PART + (size_t)(slice * 16 + rb) * PART_BLK + w * 2048; }
    if constexpr (!SSM && m == 180) { PIN(fb2[0]); PIN(fb2[1]); PIN(fb2[2]); PIN(prd); }
    if constexpr (m > 0 && bar_at(m)) {
      constexpr bool lgk0 = m == 24 || (SSM ? m == C::NM1 || m == C::NM1 - 24 : m == 204 || m == 252);
      wait_bar<S.vm[m], (lgk0 ? 0 : 15)>();
      if constexpr (pub_at<SSM>(m)) { constexpr int pc = pub_count<SSM>(m); publish(pc); }
      if constexpr (chk_at<SSM>(m)) { constexpr int n0 = N0[chk_at<SSM>(m)], n1 = N1[chk_at<SSM>(m)]; pin2(fl); await(fl, n0, n1); }
    }
    constexpr bool post = m >= 36 && (ph1 || (!SSM && m2 < 36));
    if constexpr (m + PF < C::NM) af[(m + PF) % NB] = fread(ic<m + PF>{});
    if constexpr (m + 3 < 36) xread(m + 3);
    if constexpr (SSM && m2 >= 0 && m2 < 4) hread(m2 + 16);
    if constexpr (!SSM) {
      if constexpr (m >= 192 && m < 204) { constexpr int n = m - 192; P[n >> 2][n & 3] = lds128(prd + (pslot(n >> 2) - 11) * PITCH + ((n & 3) >> 1) * PITCH + (n & 1) * 1024); }
      if constexpr (m >= 204 && m < 208) { constexpr int n = m - 204; P[3][n] = lds128(prd + (pslot(3) - 11) * PITCH + (n >> 1) * PITCH + (n & 1) * 1024); }
      if constexpr (m >= 228 && m < 232) { constexpr int n = m - 228; P[4][n] = lds128(prd + (pslot(4) - 11) * PITCH + (n >> 1) * PITCH + (n & 1) * 1024); }
      if constexpr (m >= 252 && m < 256) { constexpr int n = m - 252; P[5][n] = lds128(prd + (pslot(5) - 11) * PITCH + (n >> 1) * PITCH + (n & 1) * 1024); }
      if constexpr (ph1 && un < 5 && gn >= 30 && gn < 34) nbias[gn - 30] = lds128(biasb + (un + 1) * 256 + (gn - 30) * 32);
    }
    if constexpr (ph1) {
      if constexpr (SSM) acc = MFMA(__builtin_bit_cast(bf16x8, xf[gn]), af[m % NB], gn == 0 ? zero16 : acc);
      else {
        if constexpr (gn == 0) {
#pragma unroll
          for (int e = 0; e < 16; ++e) acc[e] = __uint_as_float(nbias[e >> 2][e & 3]); }
        acc = MFMA(af[m % NB], __builtin_bit_cast(bf16x8, xf[gn]), acc); }
    } else {
      constexpr int dp = p2_dp<SSM>(m2 / 4), ch = p2_ch<SSM>(m2 / 4);
      constexpr int bc = SSM ? h_ch(m2 % C::UNIT2) : ch, bk = SSM ? h_kk(m2 % C::UNIT2) : m & 3;
      oacc[dp] = MFMA(af[m % NB], __builtin_bit_cast(bf16x8, P[bc][bk]), (ch == 0 && (m & 3) == 0) ? zero16 : oacc[dp]);
    }
    SBAR();
    if constexpr (m >= 4 * NXP - 12 && m < 24 && (m & 1) == 0) xput(3 + (m >> 2) + 9 * ((m >> 1) & 1));
    if constexpr (ph1 && gn == 35) prev = acc;
    if constexpr (SSM) {
      if constexpr (m == 72) { int tz = threadIdx.x; PIN(tz);
#pragma unroll
        for (int k = 0; k < 4; ++k) *(LAS f32x4*)(lds0 + KSLOT * PITCH + tz * 16 + k * 8192) = (f32x4){u[0][2 * k].r, u[0][2 * k].i, u[0][2 * k + 1].r, u[0][2 * k + 1].i}; }
      if constexpr (post && gn < 35) {
        constexpr int k0 = un < NCH - 1 ? gn * 26 / 35 : gn < 12 ? gn * 16 / 12 : 16 + (gn - 12) * 10 / 23, k1 = un < NCH - 1 ? (gn + 1) * 26 / 35 : gn < 11 ? (gn + 1) * 16 / 12 : 16 + (gn - 11) * 10 / 23;
        sfor<k0, k1>([&](auto k_) { pass1(un - 1, k_, true); }); }
      if constexpr (m == C::NM1 - 24) { int tz = threadIdx.x; PIN(tz); if (w < 2) carry_out(tz, pow32(tz)); } }
    else {
      if constexpr (m >= 72 && m <= 217 && gn < 2) *(LAS u32x4*)(pwr + (pslot(un - 2) - 11) * PITCH + gn * 1024) = pn[gn];
      if constexpr (post && gn >= 2 && gn < 34) gelu_ph((gn - 2) >> 2, (gn - 2) & 3);
      if constexpr (m2 == 20 || m2 == 34) *(LAS u32x4*)(pwr + (pslot(5) - 11) * PITCH + (m2 == 34) * 1024) = pn[m2 == 34];
    }
    if constexpr (store_of<SSM>(m) >= 0) {
      constexpr int dp = store_of<SSM>(m) >> 1, e = (store_of<SSM>(m) & 1) * 8;
      u32x4 o = {pk2(oacc[dp][e], oacc[dp][e + 1]), pk2(oacc[dp][e + 2], oacc[dp][e + 3]), pk2(oacc[dp][e + 4], oacc[dp][e + 5]), pk2(oacc[dp][e + 6], oacc[dp][e + 7])};
      pst(pvo, pbase + dp * 16384 + (e >> 3) * 1024, o);
    }
    if constexpr (pf_of(m) >= 0) {
      const char* sb;
      if constexpr (SSM) { sb = sb2 + ((rb * 4 + r) * 9 + pf_of(m)) * 1152; }
      else sb = sb2 + ((rb * 8 + w) * 9 + pf_of(m)) * 384;
      touch(pfo, sb, lds0 + LDS_BYTES - 256);
    }
    if constexpr (xld_at(m) >= 0) { constexpr int dp = xld_at(m);
      if constexpr (dp == 0) { int tz = threadIdx.x; PIN(tz); xo = ((jt0 + (tz >> 4)) * D + jd0 + (tz & 15)) * 2; }
      xt[dp] = 0.f; gload_hi<dp * 128>(xt[dp], xo, xb); }
    if constexpr (m >= XP && m < XP + 5) { constexpr int k = m - XP; pinf(xt[2 * k]); if constexpr (k < 4) pinf(xt[2 * k + 1]);
      xp[k] = __uint_as_float((__float_as_uint(xt[2 * k]) >> 16) | (k < 4 ? __float_as_uint(xt[k < 4 ? 2 * k + 1 : 0]) : 0u)); pinf(xp[k]); }
    if constexpr (fld_at<SSM>(m)) gload8s(fl, (pvo & 512) * 4, flagb + w * 256);
    if constexpr (job_at<SSM>(m) >= 0) jissue(ic<JORD[job_at<SSM>(m) < 0 ? 0 : job_at<SSM>(m)]>{});
    sfor<S.fis[m], S.fis[m] + S.nis[m]>(issue);
    SBAR();
  }); });
#pragma unroll
  for (int hf = 0; hf < 2; ++hf) { const int e = hf * 8;
    u32x4 o = {pk2(oacc[8][e], oacc[8][e + 1]), pk2(oacc[8][e + 2], oacc[8][e + 3]), pk2(oacc[8][e + 4], oacc[8][e + 5]), pk2(oacc[8][e + 6], oacc[8][e + 7])};
    pst(pvo, pbase + 8 * 16384 + hf * 1024, o); }
  gload8s(fl, (pvo & 512) * 4, flagb + w * 256);
  float* const outb = kargs()->out + (size_t)t0 * D;
  tid = threadIdx.x; PIN(tid); lane = tid & 63;
  unsigned eo4 = ((jt0 + (tid >> 4)) * D + jd0 + (tid & 15)) * 4; PIN(eo4);
  const f32x2 stt = *(LAS f32x2*)(lds0 + FST + (tid >> 4) * 8);
  float xv[9], eb[9], ed[9]; unsigned ja[9], je[9];
  u32x4 sel[2];
#pragma unroll
  for (int b = 0; b < 2; ++b) { const float o = __uint_as_float(0x3f803f80u);
    const unsigned lo = __float_as_uint(selm(0.f, o, 0x100000001ull << (4 * b))), hi = __float_as_uint(selm(0.f, o, 0x200000001ull << (4 * b))); sel[b] = (u32x4){lo, lo, hi, hi}; PIN(sel[b]); }
  const unsigned jb = ((lane & 15) >> 2) * 1024 + (((4 * w + ((lane >> 4) & 1) + 32 * (lane & 1)) ^ ((lane & 12) >> 1) ^ ((lane & 1) << 3)) << 4) + ((lane >> 1) & 1) * 8;
  sfor<0, 9>([&](auto dp_) { constexpr int dp = dp_, s0 = S.jslot[2 * dp] * PITCH, s1 = S.jslot[2 * dp + 1] * PITCH;
    const unsigned so = lds0 + s0 + (lane >> 5) * (s1 - s0); ja[dp] = so + jb; je[dp] = so + (jb ^ 32); pin1(ja[dp]); pin1(je[dp]);
    const unsigned e = *(LAS unsigned*)(lds0 + ETAB + (dp * 16 + (lane & 15)) * 4); eb[dp] = lo2f(e); ed[dp] = hi2f(e); pinf(eb[dp]); pinf(ed[dp]); pinf(xp[dp >> 1]);
    xv[dp] = (dp & 1) ? hi2f(__float_as_uint(xp[dp >> 1])) : lo2f(__float_as_uint(xp[dp >> 1])); ed[dp] = rbf(rbf(rbf(xv[dp] - stt[0]) * stt[1]) * ed[dp]); pinf(ed[dp]); pinf(xv[dp]); });
  wait_bar<0, 0>();
  publish(9);
  pin2(fl); await(fl, MAXPUB, MAXPUB);
  sfor<NJIN, 2 * MAXPUB>([&](auto k_) { jissue(ic<JORD[k_]>{}); });
  auto finish = [&](auto d0_, auto d1_) { constexpr int d0 = d0_, d1 = d1_;
    bf16x8 b0[9], b1[9]; f32x16 t[9]; float uu[9], ee[9];
#pragma unroll
    for (int dp = d0; dp < d1; ++dp) { b0[dp] = cat(trd(ja[dp]), trd(ja[dp] + 4096)); b1[dp] = cat(trd(je[dp]), trd(je[dp] + 4096)); }
    SBAR();
#pragma unroll
    for (int dp = d0; dp < d1; ++dp) t[dp] = MFMA(__builtin_bit_cast(bf16x8, sel[0]), b0[dp], zero16);
#pragma unroll
    for (int dp = d0; dp < d1; ++dp) t[dp] = MFMA(__builtin_bit_cast(bf16x8, sel[1]), b1[dp], t[dp]);
    SBAR();
#pragma unroll
    for (int dp = d0; dp < d1; ++dp) uu[dp] = rbf(rbf(t[dp][0] * IBETA) + eb[dp]);
#pragma unroll
    for (int dp = d0; dp < d1; ++dp) ee[dp] = __builtin_amdgcn_exp2f(uu[dp] * -1.4426950409f);
#pragma unroll
    for (int dp = d0; dp < d1; ++dp) ee[dp] = __builtin_amdgcn_rcpf(1.f + ee[dp]);
    sfor<d0, d1>([&](auto dp_) { constexpr int dp = dp_; gstore4<dp * 256>(eo4, outb, xv[dp] + (t[dp][1] + ed[dp]) * rbf(uu[dp] * ee[dp])); });
    SBAR(); };
  constexpr int F0 = NJIN >= 8 ? NJIN / 2 : NJIN >= 7 ? 3 : 0;
  finish(ic<0>{}, ic<F0>{});
  await((u32x2){0u, 0u}, 9, 9);
  sfor<2 * MAXPUB, 18>([&](auto k_) { jissue(ic<JORD[k_]>{}); });
  wait_bar<18 - 2 * MAXPUB, 15>();
  flag_store(a.ws + WS_REPOCH + rb * 256, rtag, rtag, w | slice);
  finish(ic<F0>{}, ic<MAXPUB>{});
  wait_bar<MAXPUB - F0, 15>();
  finish(ic<MAXPUB>{}, ic<9>{});
}
#undef S
__global__ __launch_bounds__(512, 2) void k_main(Args a) {
  __shared__ __attribute__((aligned(16))) char smem[LDS_BYTES];
  const unsigned lds0 = (unsigned)(uintptr_t)smem;
  const int bid = blockIdx.x, xcd = bid & 7, loc = bid >> 3;
  const int slice = (loc & 3) * 4 + (xcd & 3), rb = (xcd >> 2) * 8 + (loc >> 2);
  const char* pc; asm volatile("s_getpc_b64 %0" : "=s"(pc));
  pc += loc * CODE_STRIDE;
  if (slice >= NFFN) {
    const int w = __builtin_amdgcn_readfirstlane(threadIdx.x >> 6);
    touch(min((int)threadIdx.x & 63, 35) * 128, (const char*)a.x + (size_t)(rb * 128 + 96) * 1152 + w * 4608, lds0 + LDS_BYTES - 256); }
  asm volatile("" ::: "memory");
  if (slice < NFFN) body<false>(lds0, slice, rb, pc); else body<true>(lds0, slice, rb, pc);
}

extern "C" void kernel_launch(void* const* d_in, const int* in_sizes, int n_in, void* d_out, int out_size, void* d_ws, size_t ws_size, hipStream_t stream) {
  Args a;
  a.x = (const u16*)d_in[0]; a.lre = (const u16*)d_in[1]; a.lim = (const u16*)d_in[2]; a.bre = (const u16*)d_in[3]; a.bim = (const u16*)d_in[4];
  a.cre = (const u16*)d_in[5]; a.cim = (const u16*)d_in[6]; a.ldt = (const u16*)d_in[7]; a.dsk = (const u16*)d_in[8];
  a.w1 = (const u16*)d_in[10]; a.b1 = (const u16*)d_in[11]; a.w2 = (const u16*)d_in[12]; a.b2 = (const u16*)d_in[13];
  a.ws = (char*)d_ws; a.out = (float*)d_out;
  k_main<<<256, 512, 0, stream>>>(a);
}
```
